```python
import torch

def _softmax(x, axis=-1):
    return torch.softmax(x, dim=axis)
def _gelu(x):
    return torch.nn.functional.gelu(x, approximate="tanh")
def _silu(x):
    return torch.nn.functional.silu(x)
def _sigmoid(x):
    return torch.sigmoid(x)
def _layernorm(x, axis=-1, eps=1e-5):
    m = x.mean(dim=axis, keepdim=True)
    v = x.var(dim=axis, keepdim=True, unbiased=False)
    return (x - m) / torch.sqrt(v + eps)
def _scatter_add(tgt, idx, src, axis=0):
    return tgt.clone().index_add_(axis, idx, src)
def _segment_sum(src, seg, n):
    out = torch.zeros((n,) + src.shape[1:], dtype=src.dtype)
    return out.index_add_(0, seg, src)
def _pad(x, pw):
    flat = tuple(v for pair in reversed(pw) for v in pair)
    return torch.nn.functional.pad(x, flat)

kernel_name = 'gated_mix__sliding_block_cached__dense_activation_relu__decode'

def setup_inputs():

    _w = (lambda a: (lambda t: t.to(torch.bfloat16) if t.is_floating_point() else t)(torch.as_tensor(a)))
    def _A_setup():
        B, Sq, D = 4, 1, 640
        Hq, Hkv, Sk, Dh = 32, 32, 4096, 192
        assert Hq % Hkv == 0, (Hq, Hkv)
        return {
            "x": _w((lambda *s: torch.randn(*s, dtype=torch.bfloat16) * 0.05)(B, Sq, D)),
            "k_cache": _w((lambda *s: torch.randn(*s, dtype=torch.bfloat16) * 3.2)(B, Hkv, Sk, Dh)),
            "v_cache": _w((lambda *s: torch.randn(*s, dtype=torch.bfloat16) * 1.6)(B, Hkv, Sk, Dh)),
            "wq": _w((lambda *s: torch.randn(*s, dtype=torch.bfloat16) * 3.2)(D, Hq * Dh) * (D ** -0.5)),
            "wo": _w((lambda *s: torch.randn(*s, dtype=torch.bfloat16) * 1.6)(Hq * Dh, D) * ((Hq * Dh) ** -0.5)),
        }
    _A_in = _A_setup()

    def _B_setup():
        B, S, D, Dff = 4, 1, 640, 1536
        return {
            "x": _w((lambda *s: torch.randn(*s, dtype=torch.bfloat16) * 0.05)(B, S, D)),
            "w1": _w((lambda *s: torch.randn(*s, dtype=torch.bfloat16) * 0.05)(D, Dff)),
            "w2": _w((lambda *s: torch.randn(*s, dtype=torch.bfloat16) * 0.05)(Dff, D)),
        }
    _B_in = _B_setup()

    return {
        "A_x": _A_in["x"],
        "A_k_cache": _A_in["k_cache"],
        "A_v_cache": _A_in["v_cache"],
        "A_wq": _A_in["wq"],
        "A_wo": _A_in["wo"],
        "B_x": _B_in["x"],
        "B_w1": _B_in["w1"],
        "B_w2": _B_in["w2"],
        "gate_w": _w((lambda *s: torch.randn(*s, dtype=torch.bfloat16) * 0.05)(640) * 16.0),
    }

def reference(A_x, A_k_cache, A_v_cache, A_wq, A_wo, B_x, B_w1, B_w2, gate_w):
    def _A_ref(x, k_cache, v_cache, wq, wo):
        B, Sq, D = x.shape
        Hq, Dh = 32, 192
        Hkv, Sk = k_cache.shape[1], k_cache.shape[2]
        W = min(4096, max(256, Sk // 4))
        rep = Hq // Hkv
        q = (lambda x, *d: x.permute(*d))((x @ wq).reshape(B, Sq, Hq, Dh), 0, 2, 1, 3)
        k = (lambda x, n, axis: torch.repeat_interleave(x, n, dim=axis))(k_cache[:, :, Sk - W:, :], rep, axis=1)
        v = (lambda x, n, axis: torch.repeat_interleave(x, n, dim=axis))(v_cache[:, :, Sk - W:, :], rep, axis=1)
        scores = torch.einsum("bhqd,bhkd->bhqk", q, k) * (Dh ** -0.5)
        p = _softmax(scores, axis=-1)
        o = torch.einsum("bhqk,bhkd->bhqd", p, v)
        return (lambda x, *d: x.permute(*d))(o, 0, 2, 1, 3).reshape(B, Sq, Hq * Dh) @ wo

    def _B_ref(x, w1, w2):
        h = torch.einsum("bsd,df->bsf", x, w1)
        zero = torch.zeros_like(h)
        h = torch.maximum(h, zero)
        return torch.einsum("bsf,fd->bsd", h, w2)

    x = A_x
    g = _sigmoid((x * gate_w).sum(-1, keepdims=True))
    a = _A_ref(x, A_k_cache, A_v_cache, A_wq, A_wo)
    b = _B_ref(x, B_w1, B_w2)
    return g * a + (1 - g) * b
```

```cpp
#include <hip/hip_runtime.h>
#include <hip/hip_bf16.h>
#include <stdint.h>

typedef unsigned u32x4 __attribute__((ext_vector_type(4)));
typedef float f32x4 __attribute__((ext_vector_type(4)));
typedef __bf16 bf16x2_t __attribute__((ext_vector_type(2)));

constexpr int NB = 4, DM = 640, NH = 32, SK = 4096, DH = 192, WIN = 1024, DFF = 1536;
constexpr int QD = NH * DH;

__device__ __forceinline__ float bf_lo(unsigned u) { return __builtin_bit_cast(float, u << 16); }
__device__ __forceinline__ float bf_hi(unsigned u) { return __builtin_bit_cast(float, u & 0xffff0000u); }
__device__ __forceinline__ float bf2f(unsigned short u) { return __builtin_bit_cast(float, (unsigned)u << 16); }
__device__ __forceinline__ unsigned short f2bf(float f) { __hip_bfloat16 b = __float2bfloat16(f); return __builtin_bit_cast(unsigned short, b); }
__device__ __forceinline__ float rbf(float f) { return bf2f(f2bf(f)); }
__device__ __forceinline__ float dot2(unsigned k, unsigned q, float acc) {
  return __builtin_amdgcn_fdot2_f32_bf16(__builtin_bit_cast(bf16x2_t, k), __builtin_bit_cast(bf16x2_t, q), acc, false);
}
template <int CTRL> __device__ __forceinline__ float dpp(float x) {
  return __builtin_bit_cast(float, __builtin_amdgcn_mov_dpp(__builtin_bit_cast(int, x), CTRL, 0xf, 0xf, true));
}

__global__ void __launch_bounds__(256) kA(const unsigned short* x, const unsigned short* wq, const unsigned short* w1, const unsigned short* gw,
                                          unsigned short* q, float* hid, float* gate) {
  __shared__ float xs[NB * DM];
  for (int i = threadIdx.x; i < NB * DM; i += 256) xs[i] = bf2f(x[i]);
  __syncthreads();
  int col = blockIdx.x * 256 + threadIdx.x;
  if (col < QD) {
    float a0 = 0, a1 = 0, a2 = 0, a3 = 0;
    for (int k = 0; k < DM; ++k) { float w = bf2f(wq[(size_t)k * QD + col]); a0 += xs[k] * w; a1 += xs[DM + k] * w; a2 += xs[2 * DM + k] * w; a3 += xs[3 * DM + k] * w; }
    q[col] = f2bf(a0); q[QD + col] = f2bf(a1); q[2 * QD + col] = f2bf(a2); q[3 * QD + col] = f2bf(a3);
  } else if (col < QD + DFF) {
    int c = col - QD;
    float a0 = 0, a1 = 0, a2 = 0, a3 = 0;
    for (int k = 0; k < DM; ++k) { float w = bf2f(w1[(size_t)k * DFF + c]); a0 += xs[k] * w; a1 += xs[DM + k] * w; a2 += xs[2 * DM + k] * w; a3 += xs[3 * DM + k] * w; }
    hid[c] = fmaxf(rbf(a0), 0.f); hid[DFF + c] = fmaxf(rbf(a1), 0.f); hid[2 * DFF + c] = fmaxf(rbf(a2), 0.f); hid[3 * DFF + c] = fmaxf(rbf(a3), 0.f);
  } else if (col < QD + DFF + NB) {
    int b = col - QD - DFF;
    float s = 0;
    for (int k = 0; k < DM; ++k) s += rbf(xs[b * DM + k] * bf2f(gw[k]));
    s = rbf(s);
    gate[b] = rbf(1.f / (1.f + __expf(-s)));
  }
}

__global__ void __launch_bounds__(512, 2) kB(const unsigned short* kc, const unsigned short* vc, const unsigned short* q, float* part) {
  __shared__ float red[8][200];
  const int wg = blockIdx.x, half = wg & 1, bh = wg >> 1, b = bh >> 5, h = bh & 31;
  const int tid = threadIdx.x, lane = tid & 63, w = tid >> 6, r = lane >> 3, c = lane & 7;
  const size_t base = ((size_t)bh * SK + (SK - WIN) + half * 512 + w * 64) * DH;
  const u32x4* kp = (const u32x4*)(kc + base + r * DH + c * 8);
  const u32x4* vp = (const u32x4*)(vc + base + r * DH + c * 8);
  u32x4 K[8][3], V[8][3];
#pragma unroll
  for (int g = 0; g < 8; ++g)
#pragma unroll
    for (int j = 0; j < 3; ++j) K[g][j] = __builtin_nontemporal_load(kp + g * (8 * DH / 8) + j * 8);
#pragma unroll
  for (int g = 0; g < 8; ++g)
#pragma unroll
    for (int j = 0; j < 3; ++j) V[g][j] = __builtin_nontemporal_load(vp + g * (8 * DH / 8) + j * 8);
  __builtin_amdgcn_sched_barrier(0);
  u32x4 qf[3];
#pragma unroll
  for (int j = 0; j < 3; ++j) qf[j] = *(const u32x4*)(q + (size_t)b * QD + h * DH + j * 64 + c * 8);
  float s[8];
#pragma unroll
  for (int g = 0; g < 8; ++g) {
    float d = 0.f;
#pragma unroll
    for (int j = 0; j < 3; ++j)
#pragma unroll
      for (int k = 0; k < 4; ++k) d = dot2(K[g][j][k], qf[j][k], d);
    d += dpp<0xB1>(d); d += dpp<0x4E>(d); d += dpp<0x141>(d);
    s[g] = d * (0.07216878364870322f * 1.4426950408889634f);
  }
  float m = s[0];
#pragma unroll
  for (int g = 1; g < 8; ++g) m = fmaxf(m, s[g]);
  m = fmaxf(m, __shfl_xor(m, 8)); m = fmaxf(m, __shfl_xor(m, 16)); m = fmaxf(m, __shfl_xor(m, 32));
  float l = 0.f, acc[24];
#pragma unroll
  for (int i = 0; i < 24; ++i) acc[i] = 0.f;
#pragma unroll
  for (int g = 0; g < 8; ++g) {
    float p = __builtin_amdgcn_exp2f(s[g] - m);
    l += p;
#pragma unroll
    for (int j = 0; j < 3; ++j)
#pragma unroll
      for (int k = 0; k < 4; ++k) { acc[j * 8 + 2 * k] = fmaf(p, bf_lo(V[g][j][k]), acc[j * 8 + 2 * k]); acc[j * 8 + 2 * k + 1] = fmaf(p, bf_hi(V[g][j][k]), acc[j * 8 + 2 * k + 1]); }
  }
#pragma unroll
  for (int i = 0; i < 24; ++i) { float t = acc[i]; t += __shfl_xor(t, 8); t += __shfl_xor(t, 16); t += __shfl_xor(t, 32); acc[i] = t; }
  l += __shfl_xor(l, 8); l += __shfl_xor(l, 16); l += __shfl_xor(l, 32);
  if (r == 0) {
#pragma unroll
    for (int j = 0; j < 3; ++j)
#pragma unroll
      for (int k = 0; k < 8; ++k) red[w][j * 64 + c * 8 + k] = acc[j * 8 + k];
    if (c == 0) { red[w][192] = m; red[w][193] = l; }
  }
  __syncthreads();
  if (tid < 192) {
    float M = red[0][192];
#pragma unroll
    for (int i = 1; i < 8; ++i) M = fmaxf(M, red[i][192]);
    float o = 0.f, L = 0.f;
#pragma unroll
    for (int i = 0; i < 8; ++i) { float e = __builtin_amdgcn_exp2f(red[i][192] - M); o = fmaf(e, red[i][tid], o); L = fmaf(e, red[i][193], L); }
    part[wg * 200 + tid] = o;
    if (tid == 0) { part[wg * 200 + 192] = M; part[wg * 200 + 193] = L; }
  }
}

__global__ void __launch_bounds__(256) kC(const float* part, const float* hid, const float* gate, const unsigned short* wo, const unsigned short* w2, unsigned short* out) {
  __shared__ float o[NB * QD];
  __shared__ float red[4][NB * 8];
  const int tid = threadIdx.x;
  for (int i = tid; i < NB * QD; i += 256) {
    int b = i / QD, rem = i % QD, h = rem / DH, d = rem % DH;
    const float* p0 = part + ((b * 32 + h) * 2) * 200; const float* p1 = p0 + 200;
    float m0 = p0[192], m1 = p1[192], M = fmaxf(m0, m1);
    float e0 = __builtin_amdgcn_exp2f(m0 - M), e1 = __builtin_amdgcn_exp2f(m1 - M);
    float L = e0 * p0[193] + e1 * p1[193];
    o[i] = rbf((e0 * p0[d] + e1 * p1[d]) / L);
  }
  __syncthreads();
  const int col0 = blockIdx.x * 8;
  float a[NB][8], bb[NB][8];
  for (int i = 0; i < NB; ++i) for (int j = 0; j < 8; ++j) { a[i][j] = 0.f; bb[i][j] = 0.f; }
  for (int k = tid; k < QD; k += 256) {
    u32x4 w = *(const u32x4*)(wo + (size_t)k * DM + col0);
    for (int i = 0; i < NB; ++i) { float ov = o[i * QD + k];
      for (int j = 0; j < 4; ++j) { a[i][2 * j] += ov * bf_lo(w[j]); a[i][2 * j + 1] += ov * bf_hi(w[j]); } }
  }
  for (int k = tid; k < DFF; k += 256) {
    u32x4 w = *(const u32x4*)(w2 + (size_t)k * DM + col0);
    for (int i = 0; i < NB; ++i) { float ov = hid[i * DFF + k];
      for (int j = 0; j < 4; ++j) { bb[i][2 * j] += ov * bf_lo(w[j]); bb[i][2 * j + 1] += ov * bf_hi(w[j]); } }
  }
  __shared__ float redA[4][32], redB[4][32];
  for (int i = 0; i < NB; ++i) for (int j = 0; j < 8; ++j) {
    float t = a[i][j], u = bb[i][j];
    for (int s = 1; s < 64; s <<= 1) { t += __shfl_xor(t, s); u += __shfl_xor(u, s); }
    if ((tid & 63) == 0) { redA[tid >> 6][i * 8 + j] = t; redB[tid >> 6][i * 8 + j] = u; }
  }
  __syncthreads();
  if (tid < 32) {
    int i = tid >> 3, j = tid & 7;
    float A = rbf(redA[0][tid] + redA[1][tid] + redA[2][tid] + redA[3][tid]);
    float B = rbf(redB[0][tid] + redB[1][tid] + redB[2][tid] + redB[3][tid]);
    float g = gate[i];
    float r = rbf(g * A) + rbf(rbf(1.f - g) * B);
    out[i * DM + col0 + j] = f2bf(r);
  }
  (void)red;
}

extern "C" void kernel_launch(void* const* d_in, const int* in_sizes, int n_in, void* d_out, int out_size, void* d_ws, size_t ws_size, hipStream_t stream) {
  const unsigned short* x = (const unsigned short*)d_in[0];
  const unsigned short* kc = (const unsigned short*)d_in[1];
  const unsigned short* vc = (const unsigned short*)d_in[2];
  const unsigned short* wq = (const unsigned short*)d_in[3];
  const unsigned short* wo = (const unsigned short*)d_in[4];
  const unsigned short* w1 = (const unsigned short*)d_in[6];
  const unsigned short* w2 = (const unsigned short*)d_in[7];
  const unsigned short* gw = (const unsigned short*)d_in[8];
  char* ws = (char*)d_ws;
  unsigned short* q = (unsigned short*)ws;
  float* hid = (float*)(ws + 65536);
  float* gate = (float*)(ws + 65536 + 32768);
  float* part = (float*)(ws + 131072);
  kA<<<(QD + DFF + NB + 255) / 256, 256, 0, stream>>>(x, wq, w1, gw, q, hid, gate);
  kB<<<256, 512, 0, stream>>>(kc, vc, q, part);
  kC<<<80, 256, 0, stream>>>(part, hid, gate, wo, w2, (unsigned short*)d_out);
}
```

```cpp
#include <hip/hip_runtime.h>
#include <hip/hip_bf16.h>
#include <stdint.h>

typedef unsigned u32x4 __attribute__((ext_vector_type(4)));
typedef unsigned u32x2 __attribute__((ext_vector_type(2)));
typedef float f32x4 __attribute__((ext_vector_type(4)));
typedef float f32x2 __attribute__((ext_vector_type(2)));
typedef short bf16x8 __attribute__((ext_vector_type(8)));
typedef __bf16 bf16x2_t __attribute__((ext_vector_type(2)));
#define GAS __attribute__((address_space(1)))
#define LAS __attribute__((address_space(3)))

#define DEPTH 4
#define WO_WAIT 8
#define T0SLEEP 16
#define T1SLEEP 6
#define T2SLEEP 4
constexpr int DM = 640, NH = 32, SK = 4096, DH = 192, WIN = 1024, DFF = 1536, QD = NH * DH;
constexpr int NTHR = 768;
constexpr int T0F_OFF = 4096;
constexpr int T0Q_OFF = T0F_OFF + 32 * 8 * 128;
constexpr int T0H_OFF = T0Q_OFF + 32 * 4 * 8 * 192 * 4;
constexpr int T1_OFF = T0H_OFF + 32 * 8 * 4 * 64 * 4;
constexpr int T2_OFF = T1_OFF + 32 * 8 * 104 * 16;
constexpr int WS_USED = T2_OFF + 8 * 32 * 160 * 16;
constexpr unsigned SPIN_CAP = 1u << 15;
constexpr int L_XS = 0;
constexpr int L_QS = 2560;
constexpr int L_MISC = 2944;
constexpr int L_HA = 3200;
constexpr int L_OA = 3776;
constexpr int L_RED = 5376;
constexpr int L_WB = 11520;
constexpr int L_W2B = 42240;
constexpr int L_END = 52480;

#define STAMP(i) do {} while (0)
#define STAMPV(i, v) do {} while (0)
#define STAMP_FLUSH() do {} while (0)

__device__ __forceinline__ float bf_lo(unsigned u) { return __builtin_bit_cast(float, u << 16); }
__device__ __forceinline__ float bf_hi(unsigned u) { return __builtin_bit_cast(float, u & 0xffff0000u); }
__device__ __forceinline__ unsigned cvtpk(float lo, float hi) {
  unsigned r; asm("v_cvt_pk_bf16_f32 %0, %1, %2" : "=v"(r) : "v"(lo), "v"(hi)); return r;
}
__device__ __forceinline__ float rbf(float f) { return bf_lo(cvtpk(f, f)); }
__device__ __forceinline__ float dot2(unsigned a, unsigned b, float acc) {
  return __builtin_amdgcn_fdot2_f32_bf16(__builtin_bit_cast(bf16x2_t, a), __builtin_bit_cast(bf16x2_t, b), acc, false);
}
template <int CTRL> __device__ __forceinline__ float dpp(float x) {
  return __builtin_bit_cast(float, __builtin_amdgcn_mov_dpp(__builtin_bit_cast(int, x), CTRL, 0xf, 0xf, true));
}
__device__ __forceinline__ float u2f(unsigned u) { return __builtin_bit_cast(float, u); }
__device__ __forceinline__ unsigned f2u(float f) { return __builtin_bit_cast(unsigned, f); }
__device__ __forceinline__ float swap32_add(float a, float b) {
  auto s = __builtin_amdgcn_permlane32_swap(f2u(a), f2u(b), false, false); return u2f(s[0]) + u2f(s[1]);
}
__device__ __forceinline__ float swap16_add(float a, float b) {
  auto s = __builtin_amdgcn_permlane16_swap(f2u(a), f2u(b), false, false); return u2f(s[0]) + u2f(s[1]);
}
__device__ __forceinline__ float swap32_max(float a) {
  auto s = __builtin_amdgcn_permlane32_swap(f2u(a), f2u(a), false, false); return fmaxf(u2f(s[0]), u2f(s[1]));
}
__device__ __forceinline__ float swap16_max(float a) {
  auto s = __builtin_amdgcn_permlane16_swap(f2u(a), f2u(a), false, false); return fmaxf(u2f(s[0]), u2f(s[1]));
}
#define SB() __builtin_amdgcn_sched_barrier(0)
#define BARRIER() asm volatile("s_waitcnt lgkmcnt(0)\n\ts_barrier" ::: "memory")
#define LGKM0() asm volatile("s_waitcnt lgkmcnt(0)" ::: "memory")
template <int N> __device__ __forceinline__ void waitvm() { asm volatile("s_waitcnt vmcnt(%0)" ::"n"(N)); }
#define LDKV(rs, vo, so) __builtin_amdgcn_raw_buffer_load_b128(rs, vo, so, 2)
__device__ __forceinline__ int opq(int v) { asm volatile("" : "+v"(v)); return v; }
#define LDSC(rs, vo) __builtin_amdgcn_raw_buffer_load_b128(rs, opq(vo), 0, 16)
#define STSC(v, rs, vo) __builtin_amdgcn_raw_buffer_store_b128(v, rs, vo, 0, 16)
__device__ __forceinline__ u32x4 ldnt(const unsigned short* p) { return __builtin_nontemporal_load((const GAS u32x4*)p); }
__device__ __forceinline__ u32x4 mkunit(float a, float b, unsigned tag) { return u32x4{f2u(a), tag, f2u(b), tag}; }
__device__ __forceinline__ bool okunit(u32x4 u, unsigned tag) { return u[1] == tag && u[3] == tag; }
__device__ __forceinline__ unsigned lds_ld(const unsigned char* lds, int off) { return *(volatile const LAS unsigned*)(lds + off); }
__device__ __forceinline__ void lds_st(unsigned char* lds, int off, unsigned v) { *(volatile LAS unsigned*)(lds + off) = v; }

__global__ void __launch_bounds__(NTHR, 3)
fused_kernel(const unsigned short* __restrict__ x, const unsigned short* __restrict__ kc, const unsigned short* __restrict__ vc,
             const unsigned short* __restrict__ wq, const unsigned short* __restrict__ wo, const unsigned short* __restrict__ w1,
             const unsigned short* __restrict__ w2, const unsigned short* __restrict__ gw, unsigned short* __restrict__ out,
             unsigned char* ws
             ) {
  __shared__ __attribute__((aligned(16))) unsigned char lds[L_END];
  const int wg = blockIdx.x;
  const int xcd = wg & 7, ii = wg >> 3, h = xcd * 4 + (ii >> 3), j = ii & 7, b = j >> 1, half = j & 1;
  const int tid = threadIdx.x, lane = tid & 63, wave = __builtin_amdgcn_readfirstlane(tid >> 6);
  const __amdgpu_buffer_rsrc_t rs = __builtin_amdgcn_make_buffer_rsrc((void*)ws, 0, WS_USED, 0x00020000);
  float* misc = (float*)(lds + L_MISC);

  if (wave >= 4) {
    const int w = wave - 4, r = lane >> 3, c = lane & 7;
    if (w == 0) STAMP(0);
    const size_t base = ((size_t)(b * NH + h) * SK + (SK - WIN) + half * 512) * DH * 2;
    const __amdgpu_buffer_rsrc_t rk = __builtin_amdgcn_make_buffer_rsrc((void*)((const char*)kc + base), 0, 512 * 384, 0x00020000);
    const __amdgpu_buffer_rsrc_t rv = __builtin_amdgcn_make_buffer_rsrc((void*)((const char*)vc + base), 0, 512 * 384, 0x00020000);
    const int voff = w * 3072 + r * 384 + c * 16;
    u32x4 K[8][3], V[8][3];
#pragma unroll
    for (int g = 0; g < 8; ++g)
#pragma unroll
      for (int t = 0; t < 3; ++t) {
        if (g * 3 + t == DEPTH) { BARRIER(); }
        if (g * 3 + t >= DEPTH) waitvm<DEPTH - 1>();
        K[g][t] = LDKV(rk, voff + t * 128, g * 24576); SB();
      }
#pragma unroll
    for (int g = 0; g < 2; ++g)
#pragma unroll
      for (int t = 0; t < 3; ++t) { waitvm<DEPTH - 1>(); V[g][t] = LDKV(rv, voff + t * 128, g * 24576); SB(); }
    if (w == 0) STAMP(2);
    for (unsigned spin = 0; spin < (1u << 22); ++spin) { if (lds_ld(lds, L_MISC + 8) != 0u) break; __builtin_amdgcn_s_sleep(1); }
    asm volatile("" ::: "memory");
    if (w == 0) STAMP(3);
    u32x4 qf[3];
#pragma unroll
    for (int t = 0; t < 3; ++t) qf[t] = *(const u32x4*)(lds + L_QS + t * 128 + c * 16);
    float s[8];
#pragma unroll
    for (int g = 0; g < 8; ++g) {
      float d0 = 0.f, d1 = 0.f, d2 = 0.f;
#pragma unroll
      for (int k = 0; k < 4; ++k) { d0 = dot2(K[g][0][k], qf[0][k], d0); d1 = dot2(K[g][1][k], qf[1][k], d1); d2 = dot2(K[g][2][k], qf[2][k], d2); }
      float d = (d0 + d1) + d2;
      asm volatile("" : "+v"(d));
      SB();
      if (g == 0) {
#pragma unroll
        for (int t = 0; t < 3; ++t) { V[2][t] = LDKV(rv, voff + t * 128, 2 * 24576); SB(); }
      }
      d += dpp<0xB1>(d); d += dpp<0x4E>(d); d += dpp<0x141>(d);
      s[g] = d * (0.07216878364870322f * 1.4426950408889634f);
    }
    float m = fmaxf(fmaxf(fmaxf(s[0], s[1]), fmaxf(s[2], s[3])), fmaxf(fmaxf(s[4], s[5]), fmaxf(s[6], s[7])));
    m = fmaxf(m, dpp<0x128>(m)); m = swap16_max(m); m = swap32_max(m);
    if (lane == 0) *(volatile LAS u32x2*)(lds + L_MISC + 160 + w * 8) = u32x2{f2u(m), 1u};
    if (w == 0) STAMP(8);
    float l = 0.f;
    f32x2 acc[12];
#pragma unroll
    for (int i = 0; i < 12; ++i) acc[i] = f32x2{0.f, 0.f};
#pragma unroll
    for (int g = 0; g < 8; ++g) {
      if (g + 3 < 8) {
        waitvm<3>();
#pragma unroll
        for (int t = 0; t < 3; ++t) { V[g + 3][t] = LDKV(rv, voff + t * 128, (g + 3) * 24576); SB(); }
        if (g + 3 == 7 && lane == 0) __hip_atomic_fetch_add((unsigned*)(misc + 1), 1u, __ATOMIC_RELAXED, __HIP_MEMORY_SCOPE_WORKGROUP);
      }
      if (g == 3) {
        u32x4 ma, mb, mc, md;
        for (unsigned spin = 0; spin < (1u << 22); ++spin) {
          ma = *(volatile const LAS u32x4*)(lds + L_MISC + 160); mb = *(volatile const LAS u32x4*)(lds + L_MISC + 176);
          mc = *(volatile const LAS u32x4*)(lds + L_MISC + 192); md = *(volatile const LAS u32x4*)(lds + L_MISC + 208);
          if ((ma[1] & ma[3] & mb[1] & mb[3] & mc[1] & mc[3] & md[1] & md[3]) != 0u) break;
          __builtin_amdgcn_s_sleep(1);
        }
        const float M = fmaxf(fmaxf(fmaxf(u2f(ma[0]), u2f(ma[2])), fmaxf(u2f(mb[0]), u2f(mb[2]))), fmaxf(fmaxf(u2f(mc[0]), u2f(mc[2])), fmaxf(u2f(md[0]), u2f(md[2]))));
        const float f = __builtin_amdgcn_exp2f(m - M);
        m = M; l *= f;
        const f32x2 f2 = {f, f};
#pragma unroll
        for (int i = 0; i < 12; ++i) acc[i] *= f2;
      }
      const float p = __builtin_amdgcn_exp2f(s[g] - m);
      l += p;
      const f32x2 p2 = {p, p};
#pragma unroll
      for (int t = 0; t < 3; ++t)
#pragma unroll
        for (int k = 0; k < 4; ++k) { const f32x2 v2 = {bf_lo(V[g][t][k]), bf_hi(V[g][t][k])}; acc[t * 4 + k] = __builtin_elementwise_fma(p2, v2, acc[t * 4 + k]); }
      asm volatile("" : "+v"(acc[0]), "+v"(acc[1]), "+v"(acc[2]), "+v"(acc[3]), "+v"(acc[4]), "+v"(acc[5]), "+v"(acc[6]), "+v"(acc[7]), "+v"(acc[8]), "+v"(acc[9]), "+v"(acc[10]), "+v"(acc[11]), "+v"(l));
      SB();
    }
    float t1[12];
#pragma unroll
    for (int a = 0; a < 12; ++a) t1[a] = swap32_add(acc[a >> 1][a & 1], acc[(a + 12) >> 1][a & 1]);
    float t2[6];
#pragma unroll
    for (int a = 0; a < 6; ++a) t2[a] = swap16_add(t1[a], t1[a + 6]);
#pragma unroll
    for (int a = 0; a < 6; ++a) t2[a] += dpp<0x128>(t2[a]);
    l += dpp<0x128>(l); l = swap16_add(l, l); l = swap32_add(l, l);
    {
      const int rho = lane >> 4;
      if ((lane & 8) == 0) {
        float* red = (float*)(lds + L_RED) + w * 192;
#pragma unroll
        for (int i2 = 0; i2 < 3; ++i2) { const int a = 6 * rho + 2 * i2; *(f32x2*)(red + (a >> 3) * 64 + c * 8 + (a & 7)) = f32x2{t2[2 * i2], t2[2 * i2 + 1]}; }
      }
      if (lane == 0) misc[24 + w] = l;
    }
    LGKM0();
    if (lane == 0) __hip_atomic_fetch_add((unsigned*)(misc + 3), 1u, __ATOMIC_RELAXED, __HIP_MEMORY_SCOPE_WORKGROUP);
    if (w == 0) STAMP(4);
    const unsigned tag = f2u(misc[0]);
    if (w >= 6) {
      for (unsigned spin = 0; spin < (1u << 22); ++spin) { if (lds_ld(lds, L_MISC + 12) >= 8u) break; }
      asm volatile("" ::: "memory");
      const int u = (w - 6) * 64 + lane;
      if (u < 96) {
        const float* red = (const float*)(lds + L_RED) + 2 * u;
        f32x2 o = *(const f32x2*)red;
#pragma unroll
        for (int ww = 1; ww < 8; ++ww) o += *(const f32x2*)(red + ww * 192);
        STSC(mkunit(o[0], o[1], tag), rs, T1_OFF + ((h * 8 + j) * 104 + u) * 16);
      } else if (u < 104) {
        const f32x4 la = *(const f32x4*)(misc + 24), lb = *(const f32x4*)(misc + 28);
        const float L = ((la[0] + la[1]) + (la[2] + la[3])) + ((lb[0] + lb[1]) + (lb[2] + lb[3]));
        STSC(mkunit(m, L, tag), rs, T1_OFF + ((h * 8 + j) * 104 + u) * 16);
      }
      if (w == 6) STAMP(11);
    } else {
      const int it = w * 64 + lane, bb = it / 96, p = it - bb * 96;
      const int o0 = T1_OFF + ((h * 8 + 2 * bb) * 104) * 16, o1 = o0 + 104 * 16;
      u32x4 a0 = {0, 0, 0, 0}, a1 = a0, m0 = a0, m1 = a0;
      bool ok0 = false, ok1 = false;
      for (unsigned spin = 0;; ++spin) {
        if (!ok0) { a0 = LDSC(rs, o0 + p * 16); m0 = LDSC(rs, o0 + 96 * 16); }
        if (!ok1) { a1 = LDSC(rs, o1 + p * 16); m1 = LDSC(rs, o1 + 96 * 16); }
        ok0 = okunit(a0, tag) && okunit(m0, tag); ok1 = okunit(a1, tag) && okunit(m1, tag);
        if (__all(ok0 && ok1) || spin > SPIN_CAP) break;
        __builtin_amdgcn_s_sleep(T1SLEEP);
      }
      const float M0 = u2f(m0[0]), M1 = u2f(m1[0]), M = fmaxf(M0, M1);
      const float e0 = __builtin_amdgcn_exp2f(M0 - M), e1 = __builtin_amdgcn_exp2f(M1 - M);
      const float inv = __builtin_amdgcn_rcpf(e0 * u2f(m0[2]) + e1 * u2f(m1[2]));
      const float f0 = e0 * inv, f1 = e1 * inv;
      const unsigned pk = cvtpk(f0 * u2f(a0[0]) + f1 * u2f(a1[0]), f0 * u2f(a0[2]) + f1 * u2f(a1[2]));
      *(unsigned*)(lds + L_OA + bb * 400 + p * 4) = pk;
      if (w == 0) STAMP(12);
    }
    BARRIER();
    if (w == 0) STAMP(5);
    if (w < 5) {
      const int nt = w;
      f32x4 aa = {0.f, 0.f, 0.f, 0.f}, ab = {0.f, 0.f, 0.f, 0.f};
      bf16x8 af[6], bf[6], haf[2], hbf[2];
      const int slot = (lane & 56) | ((lane & 7) ^ ((2 * nt + ((lane >> 3) & 1)) & 7));
#pragma unroll
      for (int ks = 0; ks < 6; ++ks) {
        af[ks] = *(const bf16x8*)(lds + L_OA + (lane & 3) * 400 + ks * 64 + (lane >> 4) * 16);
        bf[ks] = *(const bf16x8*)(lds + L_WB + ((ks * 5 + nt) * 64 + slot) * 16);
      }
#pragma unroll
      for (int ks = 0; ks < 2; ++ks) {
        haf[ks] = *(const bf16x8*)(lds + L_HA + (lane & 3) * 144 + ks * 64 + (lane >> 4) * 16);
        hbf[ks] = *(const bf16x8*)(lds + L_W2B + ((ks * 5 + nt) * 64 + slot) * 16);
      }
      f32x4 aa2 = {0.f, 0.f, 0.f, 0.f};
#pragma unroll
      for (int ks = 0; ks < 3; ++ks) { aa = __builtin_amdgcn_mfma_f32_16x16x32_bf16(af[ks], bf[ks], aa, 0, 0, 0); aa2 = __builtin_amdgcn_mfma_f32_16x16x32_bf16(af[ks + 3], bf[ks + 3], aa2, 0, 0, 0); }
#pragma unroll
      for (int ks = 0; ks < 2; ++ks) ab = __builtin_amdgcn_mfma_f32_16x16x32_bf16(haf[ks], hbf[ks], ab, 0, 0, 0);
      if (lane < 16) {
        const f32x4 ga = *(const f32x4*)(misc + 32), gb = *(const f32x4*)(misc + 36);
        const int cc = 16 * nt + lane;
#pragma unroll
        for (int bp = 0; bp < 2; ++bp) {
          const float v0 = ga[2 * bp] * (aa[2 * bp] + aa2[2 * bp]) + gb[2 * bp] * ab[2 * bp];
          const float v1 = ga[2 * bp + 1] * (aa[2 * bp + 1] + aa2[2 * bp + 1]) + gb[2 * bp + 1] * ab[2 * bp + 1];
          STSC(mkunit(v0, v1, tag), rs, T2_OFF + ((j * 32 + h) * 160 + bp * 80 + cc) * 16);
        }
      }
      if (w == 0) STAMP(6);
    } else if (w == 5) {
      const int o0 = T2_OFF + ((j * 32 + (lane & 31)) * 160 + 5 * h + (lane >> 5)) * 16;
      u32x4 u0 = {0, 0, 0, 0}, u1 = u0, u2 = u0;
      bool ok = false;
      for (unsigned spin = 0;; ++spin) {
        if (!ok) { u0 = LDSC(rs, o0); u1 = LDSC(rs, o0 + 32); u2 = LDSC(rs, o0 + (lane < 32 ? 64 : 0)); }
        ok = okunit(u0, tag) && okunit(u1, tag) && okunit(u2, tag);
        if (__all(ok) || spin > SPIN_CAP) break;
        __builtin_amdgcn_s_sleep(T2SLEEP);
      }
      float v[6] = {u2f(u0[0]), u2f(u0[2]), u2f(u1[0]), u2f(u1[2]), u2f(u2[0]), u2f(u2[2])};
#pragma unroll
      for (int i = 0; i < 6; ++i) {
        float t = v[i];
        t += dpp<0xB1>(t); t += dpp<0x4E>(t); t += dpp<0x141>(t); t += dpp<0x140>(t);
        v[i] = swap16_add(t, t);
      }
      if ((lane & 31) == 0) {
#pragma unroll
        for (int i = 0; i < 3; ++i) {
          const int p = 2 * i + (lane >> 5);
          if (p < 5) {
            const int u = 5 * h + p, bp = u >= 80 ? 1 : 0, cc = u - bp * 80;
            const unsigned pk = cvtpk(v[2 * i], v[2 * i + 1]);
            out[(2 * bp) * DM + 80 * j + cc] = (unsigned short)(pk & 0xffffu);
            out[(2 * bp + 1) * DM + 80 * j + cc] = (unsigned short)(pk >> 16);
          }
        }
      }
      if (wg == 0 && lane == 0) __hip_atomic_store((GAS unsigned*)ws, tag, __ATOMIC_RELAXED, __HIP_MEMORY_SCOPE_AGENT);
      STAMP(7);
    }
    STAMP_FLUSH();
  } else {
    __builtin_amdgcn_s_setprio(3);
    const int hw = wave, r = lane >> 3, c = lane & 7;
    const unsigned tag = __hip_atomic_load((GAS unsigned*)ws, __ATOMIC_RELAXED, __HIP_MEMORY_SCOPE_AGENT) + 1u;
    unsigned xp[3];
#pragma unroll
    for (int i = 0; i < 3; ++i) { int id = lane + 64 * i; id = id < 160 ? id : 159; const int bb = id / 40, kp = id - bb * 40;
      xp[i] = *(const GAS unsigned*)(x + bb * DM + 80 * j + 2 * kp); }
    SB();
    u32x4 W[10];
    {
      const int ce = (hw == 3 && c >= 6) ? 5 : c;
      const unsigned short* p0 = hw < 3 ? (wq + (size_t)(80 * j + 2 * r) * QD + h * DH + hw * 64 + ce * 8) : (w1 + (size_t)(80 * j + 2 * r) * DFF + h * 48 + ce * 8);
      const int rs1 = hw < 3 ? QD : DFF;
#pragma unroll
      for (int m = 0; m < 5; ++m)
#pragma unroll
        for (int sub = 0; sub < 2; ++sub) { W[2 * m + sub] = ldnt(p0 + (size_t)((16 * m + sub) * rs1)); SB(); }
    }
    if (hw == 0) lds_st(lds, L_MISC + lane * 4, 0u);
    BARRIER();
#pragma unroll
    for (int i = 0; i < 3; ++i) { const int id = lane + 64 * i; if (id < 160) { const int bb = id / 40, kp = id - bb * 40; *(unsigned*)(lds + L_XS + hw * 640 + kp * 16 + bb * 4) = xp[i]; } }
    if (hw == 0 && lane == 0) misc[0] = u2f(tag);
    float acc[32];
#pragma unroll
    for (int i = 0; i < 32; ++i) acc[i] = 0.f;
#pragma unroll
    for (int m = 0; m < 5; ++m) {
      const u32x4 xq = *(const u32x4*)(lds + L_XS + hw * 640 + (8 * m + r) * 16);
#pragma unroll
      for (int d = 0; d < 4; ++d) {
        const unsigned lo = __builtin_amdgcn_perm(W[2 * m + 1][d], W[2 * m][d], 0x05040100u);
        const unsigned hi = __builtin_amdgcn_perm(W[2 * m + 1][d], W[2 * m][d], 0x07060302u);
#pragma unroll
        for (int bb = 0; bb < 4; ++bb) { acc[bb * 8 + 2 * d] = dot2(lo, xq[bb], acc[bb * 8 + 2 * d]); acc[bb * 8 + 2 * d + 1] = dot2(hi, xq[bb], acc[bb * 8 + 2 * d + 1]); }
      }
    }
    {
      float t[16], u[8];
#pragma unroll
      for (int i = 0; i < 16; ++i) t[i] = swap32_add(acc[i], acc[i + 16]);
#pragma unroll
      for (int i = 0; i < 8; ++i) u[i] = swap16_add(t[i], t[i + 8]);
#pragma unroll
      for (int i = 0; i < 8; ++i) u[i] += dpp<0x128>(u[i]);
      const int bb = lane >> 4, hi4 = (lane >> 3) & 1;
      const f32x4 v = {hi4 ? u[4] : u[0], hi4 ? u[5] : u[1], hi4 ? u[6] : u[2], hi4 ? u[7] : u[3]};
      const int off = hw < 3 ? (T0Q_OFF + (((h * 4 + bb) * 8 + j) * 192 + hw * 64 + c * 8 + hi4 * 4) * 4)
                             : (T0H_OFF + (((h * 8 + j) * 4 + bb) * 64 + c * 8 + hi4 * 4) * 4);
      if (hw == 0) STAMP(9);
      STSC(__builtin_bit_cast(u32x4, v), rs, off);
    }
    asm volatile("s_waitcnt vmcnt(0)" ::: "memory");
    if (lane == 0) lds_st(lds, L_MISC + 16 + hw * 4, 1u);
    LGKM0();
    {
      const u32x4 dn = *(volatile const LAS u32x4*)(lds + L_MISC + 16);
      if ((dn[0] & dn[1] & dn[2] & dn[3]) != 0u && lane < 8) STSC((u32x4{tag, tag, tag, tag}), rs, T0F_OFF + (h * 8 + j) * 128 + lane * 16);
    }
    if (hw == 0) STAMP(10);
    u32x4 G[10], W2[8];
    if (hw < 2) {
      const int offs = T0F_OFF + (h * 8 + (lane & 7)) * 128;
      for (unsigned spin = 0;; ++spin) {
        const u32x4 sn = LDSC(rs, offs);
        if (__all(okunit(sn, tag)) || spin > SPIN_CAP) break;
        __builtin_amdgcn_s_sleep(T0SLEEP);
      }
      __builtin_amdgcn_fence(__ATOMIC_ACQUIRE, "wavefront");
      if (hw == 0) STAMP(15);
      const int l48 = lane < 48 ? lane : 47;
      const int hb = l48 / 12, hq = l48 - hb * 12;
      const int off0 = hw == 0 ? (T0Q_OFF + ((h * 4 + b) * 8 * 192 + l48 * 4) * 4) : (T0H_OFF + ((h * 8 * 4 + hb) * 64 + hq * 4) * 4);
      const int jstr = hw == 0 ? 768 : 1024;
      u32x4 U[8];
#pragma unroll
      for (int jj = 0; jj < 8; ++jj) U[jj] = LDSC(rs, off0 + jj * jstr);
      f32x4 v = __builtin_bit_cast(f32x4, U[0]);
#pragma unroll
      for (int jj = 1; jj < 8; ++jj) v += __builtin_bit_cast(f32x4, U[jj]);
      if (hw == 0) {
        if (lane < 48) *(u32x2*)(lds + L_QS + lane * 8) = u32x2{cvtpk(v[0], v[1]), cvtpk(v[2], v[3])};
        LGKM0();
        if (lane == 0) lds_st(lds, L_MISC + 8, 1u);
        STAMP(1);
      } else {
        if (lane < 48) *(u32x2*)(lds + L_HA + hb * 144 + hq * 8) = u32x2{cvtpk(fmaxf(v[0], 0.f), fmaxf(v[1], 0.f)), cvtpk(fmaxf(v[2], 0.f), fmaxf(v[3], 0.f))};
        if (lane < 32) *(unsigned*)(lds + L_HA + (lane >> 3) * 144 + (24 + (lane & 7)) * 4) = 0u;
      }
    } else if (hw == 2) {
      const int l16 = lane < 16 ? lane + 64 : lane;
      G[0] = *(const GAS u32x4*)(gw + lane * 8); G[1] = *(const GAS u32x4*)(gw + l16 * 8);
#pragma unroll
      for (int bb = 0; bb < 4; ++bb) { G[2 + 2 * bb] = *(const GAS u32x4*)(x + bb * DM + lane * 8); G[3 + 2 * bb] = *(const GAS u32x4*)(x + bb * DM + l16 * 8); }
#pragma unroll
      for (int bb = 0; bb < 4; ++bb) {
        float s0 = 0.f, s1 = 0.f;
#pragma unroll
        for (int d = 0; d < 4; ++d) {
          s0 += rbf(bf_lo(G[2 + 2 * bb][d]) * bf_lo(G[0][d])); s0 += rbf(bf_hi(G[2 + 2 * bb][d]) * bf_hi(G[0][d]));
          s1 += rbf(bf_lo(G[3 + 2 * bb][d]) * bf_lo(G[1][d])); s1 += rbf(bf_hi(G[3 + 2 * bb][d]) * bf_hi(G[1][d]));
        }
        float t = s0 + (lane < 16 ? s1 : 0.f);
        t += dpp<0xB1>(t); t += dpp<0x4E>(t); t += dpp<0x141>(t); t += dpp<0x140>(t);
        t = swap16_add(t, t); t = swap32_add(t, t);
        t = rbf(t);
        const float g = rbf(1.f / (1.f + __expf(-t)));
        if (lane == 0) { misc[32 + bb] = g; misc[36 + bb] = rbf(1.f - g); }
      }
    } else {
      const int l60 = lane < 60 ? lane : 59, rb = l60 / 10, cq = l60 - rb * 10;
      const unsigned short* p2 = w2 + (size_t)(48 * h + 8 * rb) * DM + 80 * j + cq * 8;
#pragma unroll
      for (int i = 0; i < 8; ++i) W2[i] = ldnt(p2 + i * DM);
      if (lane < 60) {
        const int ks = rb >> 2, kg = rb & 3, nt = cq >> 1;
        const int dst = L_W2B + ((ks * 5 + nt) * 64 + kg * 16 + (cq & 1) * 8 + (cq & 7)) * 16;
#pragma unroll
        for (int cc = 0; cc < 8; ++cc) {
          const unsigned sel = (cc & 1) ? 0x07060302u : 0x05040100u;
          u32x4 o;
#pragma unroll
          for (int kk = 0; kk < 4; ++kk) o[kk] = __builtin_amdgcn_perm(W2[2 * kk + 1][cc >> 1], W2[2 * kk][cc >> 1], sel);
          *(u32x4*)(lds + (dst ^ (cc * 16))) = o;
        }
      }
#pragma unroll
      for (int i = 0; i < 3; ++i) { const int id = lane + 64 * i; if (id < 160) *(u32x4*)(lds + L_W2B + ((5 + id / 32) * 64 + 32 + (id & 31)) * 16) = u32x4{0u, 0u, 0u, 0u}; }
    }
    for (unsigned spin = 0; spin < (1u << 22); ++spin) {
      if (lds_ld(lds, L_MISC + 4) >= WO_WAIT) break;
      __builtin_amdgcn_s_sleep(4);
    }
    if (hw == 0) STAMP(14);
    u32x4 WO[8];
    const int l60 = lane < 60 ? lane : 59, rbl = l60 / 10, cq = l60 - rbl * 10;
    {
      const unsigned short* po = wo + (size_t)(192 * h + 8 * (6 * hw + rbl)) * DM + 80 * j + cq * 8;
#pragma unroll
      for (int i = 0; i < 8; ++i) { if (i >= 3) waitvm<2>(); WO[i] = ldnt(po + i * DM); SB(); }
    }
    if (lane < 60) {
      const int rb = 6 * hw + rbl, ks = rb >> 2, kg = rb & 3, nt = cq >> 1;
      const int dst = L_WB + ((ks * 5 + nt) * 64 + kg * 16 + (cq & 1) * 8 + (cq & 7)) * 16;
#pragma unroll
      for (int cc = 0; cc < 8; ++cc) {
        const unsigned sel = (cc & 1) ? 0x07060302u : 0x05040100u;
        u32x4 o;
#pragma unroll
        for (int kk = 0; kk < 4; ++kk) o[kk] = __builtin_amdgcn_perm(WO[2 * kk + 1][cc >> 1], WO[2 * kk][cc >> 1], sel);
        *(u32x4*)(lds + (dst ^ (cc * 16))) = o;
      }
    }
    if (hw == 0) STAMP(13);
    BARRIER();
    STAMP_FLUSH();
  }
}

extern "C" void kernel_launch(void* const* d_in, const int* in_sizes, int n_in, void* d_out, int out_size, void* d_ws, size_t ws_size, hipStream_t stream) {
  fused_kernel<<<256, NTHR, 0, stream>>>((const unsigned short*)d_in[0], (const unsigned short*)d_in[1], (const unsigned short*)d_in[2],
      (const unsigned short*)d_in[3], (const unsigned short*)d_in[4], (const unsigned short*)d_in[6], (const unsigned short*)d_in[7],
      (const unsigned short*)d_in[8], (unsigned short*)d_out, (unsigned char*)d_ws
      );
}
```

```cpp
#include <hip/hip_runtime.h>
#include <hip/hip_bf16.h>
#include <stdint.h>

typedef unsigned u32x4 __attribute__((ext_vector_type(4)));
typedef unsigned u32x2 __attribute__((ext_vector_type(2)));
typedef float f32x4 __attribute__((ext_vector_type(4)));
typedef float f32x2 __attribute__((ext_vector_type(2)));
typedef short bf16x8 __attribute__((ext_vector_type(8)));
typedef __bf16 bf16x2_t __attribute__((ext_vector_type(2)));
#define GAS __attribute__((address_space(1)))
#define LAS __attribute__((address_space(3)))

#define DEPTH 6
#define WO_WAIT 8
#ifndef NVPRE
#define NVPRE 4
#endif
#define PVW 3
#define T0DELAY 40
#define T0SLEEP 12
#define T1POL 2
#define T2POL 1
#define T1SLEEP 4
#define T2SLEEP 4
#define LDAUX 16
#define STAUX 16
#define T0POL 1
#define T0AUX 16
#define PACE 11
#define PACE2 0
#define T1DIR 1
#define T1SLEEP2 1
#define POLLGAP 8
constexpr int DM = 640, NH = 32, SK = 4096, DH = 192, WIN = 1024, DFF = 1536, QD = NH * DH;
constexpr int NTHR = 768;
constexpr int T0Q_OFF = 4096;
constexpr int T0H_OFF = T0Q_OFF + 32 * 4 * 8 * 96 * 16;
constexpr int T1_OFF = T0H_OFF + 32 * 8 * 128 * 16;
constexpr int T2_OFF = T1_OFF + 32 * 8 * 104 * 16;
constexpr int WS_USED = T2_OFF + 8 * 32 * 160 * 16;
constexpr unsigned SPIN_CAP = 1u << 15;
constexpr unsigned LSPIN = 1u << 22;
constexpr int L_XS = 0;
constexpr int L_QS = 2560;
constexpr int L_MISC = 2944;
constexpr int L_HA = 3200;
constexpr int L_OA = 3776;
constexpr int L_RED = 5376;
constexpr int L_WB = 11520;
constexpr int L_W2B = 42240;
constexpr int L_END = 52480;
static_assert(L_WB % 128 == 0 && L_W2B % 128 == 0, "swizzle");
constexpr int C_TAG = 0, C_VISS = 4, C_Q = 8, C_PV = 12, C_TILE = 16, C_FFN = 20, C_OA = 24, C_T0 = 28, C_LS = 96, C_G = 128, C_OMG = 144, C_MX = 160;

#define STAMP(i) do {} while (0)
#define STAMP_FLUSH() do {} while (0)

__device__ __forceinline__ float bf_lo(unsigned u) { return __builtin_bit_cast(float, u << 16); }
__device__ __forceinline__ float bf_hi(unsigned u) { return __builtin_bit_cast(float, u & 0xffff0000u); }
__device__ __forceinline__ unsigned cvtpk(float lo, float hi) {
  unsigned r; asm("v_cvt_pk_bf16_f32 %0, %1, %2" : "=v"(r) : "v"(lo), "v"(hi)); return r;
}
__device__ __forceinline__ float rbf(float f) { return bf_lo(cvtpk(f, f)); }
__device__ __forceinline__ float dot2(unsigned a, unsigned b, float acc) {
  return __builtin_amdgcn_fdot2_f32_bf16(__builtin_bit_cast(bf16x2_t, a), __builtin_bit_cast(bf16x2_t, b), acc, false);
}
template <int CTRL> __device__ __forceinline__ float dpp(float x) {
  return __builtin_bit_cast(float, __builtin_amdgcn_mov_dpp(__builtin_bit_cast(int, x), CTRL, 0xf, 0xf, true));
}
__device__ __forceinline__ float u2f(unsigned u) { return __builtin_bit_cast(float, u); }
__device__ __forceinline__ unsigned f2u(float f) { return __builtin_bit_cast(unsigned, f); }
__device__ __forceinline__ float swap32_add(float a, float b) {
  auto s = __builtin_amdgcn_permlane32_swap(f2u(a), f2u(b), false, false); return u2f(s[0]) + u2f(s[1]);
}
__device__ __forceinline__ float swap16_add(float a, float b) {
  auto s = __builtin_amdgcn_permlane16_swap(f2u(a), f2u(b), false, false); return u2f(s[0]) + u2f(s[1]);
}
__device__ __forceinline__ float swap32_max(float a) {
  auto s = __builtin_amdgcn_permlane32_swap(f2u(a), f2u(a), false, false); return fmaxf(u2f(s[0]), u2f(s[1]));
}
__device__ __forceinline__ float swap16_max(float a) {
  auto s = __builtin_amdgcn_permlane16_swap(f2u(a), f2u(a), false, false); return fmaxf(u2f(s[0]), u2f(s[1]));
}
#define SB() __builtin_amdgcn_sched_barrier(0)
#define BARRIER() asm volatile("s_waitcnt lgkmcnt(0)\n\ts_barrier" ::: "memory")
#define LGKM0() asm volatile("s_waitcnt lgkmcnt(0)" ::: "memory")
#define CBAR() asm volatile("" ::: "memory")
template <int N> __device__ __forceinline__ void waitvm() { asm volatile("s_waitcnt vmcnt(%0)" ::"n"(N)); }
#define LDKV(rs, vo, so) __builtin_amdgcn_raw_buffer_load_b128(rs, vo, so, 2)
__device__ __forceinline__ int opq(int v) { asm volatile("" : "+v"(v)); return v; }
#define LDSC(rs, vo) __builtin_amdgcn_raw_buffer_load_b128(rs, opq(vo), 0, LDAUX)
#define STSC(v, rs, vo) __builtin_amdgcn_raw_buffer_store_b128(v, rs, vo, 0, STAUX)
__device__ __forceinline__ u32x4 ldnt(const unsigned short* p) { return __builtin_nontemporal_load((const GAS u32x4*)p); }
__device__ __forceinline__ u32x4 mkunit(float a, float b, unsigned tag) { return u32x4{f2u(a), tag, f2u(b), tag}; }
__device__ __forceinline__ bool okunit(u32x4 u, unsigned tag) { return u[1] == tag && u[3] == tag; }
__device__ __forceinline__ unsigned ctl_ld(const unsigned char* lds, int off) { return *(volatile const LAS unsigned*)(lds + L_MISC + off); }
__device__ __forceinline__ void ctl_st(unsigned char* lds, int off, unsigned v) { *(volatile LAS unsigned*)(lds + L_MISC + off) = v; }
__device__ __forceinline__ void ctl_inc(unsigned char* lds, int off, int lane) {
  LGKM0();
  if (lane == 0) __hip_atomic_fetch_add((LAS unsigned*)(lds + L_MISC + off), 1u, __ATOMIC_RELAXED, __HIP_MEMORY_SCOPE_WORKGROUP);
}
template <int SLEEP> __device__ __forceinline__ void ctl_wait(const unsigned char* lds, int off, unsigned want) {
  for (unsigned spin = 0; spin < LSPIN; ++spin) { if (ctl_ld(lds, off) >= want) break; if (SLEEP) __builtin_amdgcn_s_sleep(SLEEP); }
  CBAR();
}

__device__ __forceinline__ void outproj_tile(unsigned char* lds, __amdgpu_buffer_rsrc_t rs, int nt, int lane, int h, int j, unsigned tag) {
  ctl_wait<1>(lds, C_TILE, 4u);
  ctl_wait<1>(lds, C_FFN, 2u);
  const int slot = (lane & 56) | ((lane & 7) ^ ((2 * nt + ((lane >> 3) & 1)) & 7));
  bf16x8 bf[6], haf[2], hbf[2], af[6];
#pragma unroll
  for (int ks = 0; ks < 6; ++ks) bf[ks] = *(const bf16x8*)(lds + L_WB + ((ks * 5 + nt) * 64 + slot) * 16);
#pragma unroll
  for (int ks = 0; ks < 2; ++ks) {
    haf[ks] = *(const bf16x8*)(lds + L_HA + (lane & 3) * 144 + ks * 64 + (lane >> 4) * 16);
    hbf[ks] = *(const bf16x8*)(lds + L_W2B + ((ks * 5 + nt) * 64 + slot) * 16);
  }
  const f32x4 ga = *(const f32x4*)(lds + L_MISC + C_G), gb = *(const f32x4*)(lds + L_MISC + C_OMG);
  f32x4 ab = {0.f, 0.f, 0.f, 0.f};
#pragma unroll
  for (int ks = 0; ks < 2; ++ks) ab = __builtin_amdgcn_mfma_f32_16x16x32_bf16(haf[ks], hbf[ks], ab, 0, 0, 0);
  ab *= gb;
  const int off = T2_OFF + ((j * 32 + h) * 160 + 16 * nt + lane) * 16;
  const int aoff = L_OA + (lane & 3) * 400 + (lane >> 4) * 16;
  asm volatile("" : "+v"(ab), "+v"(bf[0]), "+v"(bf[1]), "+v"(bf[2]), "+v"(bf[3]), "+v"(bf[4]), "+v"(bf[5]));
  ctl_wait<0>(lds, C_OA, 6u);
#pragma unroll
  for (int ks = 0; ks < 6; ++ks) af[ks] = *(const bf16x8*)(lds + aoff + ks * 64);
  f32x4 aa = {0.f, 0.f, 0.f, 0.f}, aa2 = {0.f, 0.f, 0.f, 0.f};
#pragma unroll
  for (int ks = 0; ks < 3; ++ks) { aa = __builtin_amdgcn_mfma_f32_16x16x32_bf16(af[ks], bf[ks], aa, 0, 0, 0); aa2 = __builtin_amdgcn_mfma_f32_16x16x32_bf16(af[ks + 3], bf[ks + 3], aa2, 0, 0, 0); }
  if (lane < 16) {
    const f32x4 v = ga * (aa + aa2) + ab;
    STSC(mkunit(v[0], v[1], tag), rs, off); STSC(mkunit(v[2], v[3], tag), rs, off + 80 * 16);
  }
}

__device__ __forceinline__ void final_part(unsigned char* lds, __amdgpu_buffer_rsrc_t rs, int i, int lane, int h, int j, unsigned tag, unsigned short* out) {
  const int p = 2 * i + (lane >> 5);
  const int o0 = T2_OFF + ((j * 32 + (lane & 31)) * 160 + 5 * h + p) * 16;
  const int u5 = 5 * h + p, bp = u5 >= 80 ? 1 : 0, cc = u5 - bp * 80;
  unsigned short* dst = out + (2 * bp) * DM + 80 * j + cc;
  ctl_wait<1>(lds, C_OA, 6u);
  u32x4 R = {0u, 0u, 0u, 0u};
  bool done = p >= 5;
  for (unsigned spin = 0; spin < SPIN_CAP; ++spin) {
    if (!done) { R = LDSC(rs, o0); done = okunit(R, tag); }
    if (__all(done)) break;
    __builtin_amdgcn_s_sleep(T2SLEEP);
  }
  float v0 = u2f(R[0]), v1 = u2f(R[2]);
  v0 += dpp<0xB1>(v0); v1 += dpp<0xB1>(v1); v0 += dpp<0x4E>(v0); v1 += dpp<0x4E>(v1);
  v0 += dpp<0x141>(v0); v1 += dpp<0x141>(v1); v0 += dpp<0x140>(v0); v1 += dpp<0x140>(v1);
  const float t = swap16_add(v0, v1);
  const unsigned pk = cvtpk(t, t);
  if ((lane & 15) == 0 && p < 5) dst[(lane & 16) ? DM : 0] = (unsigned short)(pk & 0xffffu);
}

__global__ void __launch_bounds__(NTHR, 3)
fused_kernel(const unsigned short* __restrict__ x, const unsigned short* __restrict__ kc, const unsigned short* __restrict__ vc,
             const unsigned short* __restrict__ wq, const unsigned short* __restrict__ wo, const unsigned short* __restrict__ w1,
             const unsigned short* __restrict__ w2, const unsigned short* __restrict__ gw, unsigned short* __restrict__ out,
             unsigned char* ws
             ) {
  __shared__ __attribute__((aligned(128))) unsigned char lds[L_END];
  const int wg = blockIdx.x;
  const int xcd = wg & 7, ii = wg >> 3, h = xcd * 4 + (ii >> 3), j = ii & 7, b = j >> 1, half = j & 1;
  const int tid = threadIdx.x, lane = tid & 63, wave = __builtin_amdgcn_readfirstlane(tid >> 6);
  const __amdgpu_buffer_rsrc_t rs = __builtin_amdgcn_make_buffer_rsrc((void*)ws, 0, WS_USED, 0x00020000);

  if (wave >= 4) {
    const int w = wave - 4, r = lane >> 3, c = lane & 7;
    if (w == 0) STAMP(0);
    const size_t base = ((size_t)(b * NH + h) * SK + (SK - WIN) + half * 512) * DH * 2;
    const __amdgpu_buffer_rsrc_t rk = __builtin_amdgcn_make_buffer_rsrc((void*)((const char*)kc + base), 0, 512 * 384, 0x00020000);
    const __amdgpu_buffer_rsrc_t rv = __builtin_amdgcn_make_buffer_rsrc((void*)((const char*)vc + base), 0, 512 * 384, 0x00020000);
    const int voff = w * 3072 + r * 384 + c * 16;
    u32x4 K[8][3], V[8][3];
#pragma unroll
    for (int g = 0; g < 8; ++g)
#pragma unroll
      for (int t = 0; t < 3; ++t) {
        if (g * 3 + t == DEPTH) { BARRIER(); }
        if (g * 3 + t >= DEPTH) waitvm<DEPTH - 1>();
        K[g][t] = LDKV(rk, voff + t * 128, g * 24576); if (PACE) __builtin_amdgcn_s_sleep(PACE); SB();
      }
#pragma unroll
    for (int g = 0; g < NVPRE; ++g)
#pragma unroll
      for (int t = 0; t < 3; ++t) { waitvm<DEPTH - 1>(); V[g][t] = LDKV(rv, voff + t * 128, g * 24576); if (PACE) __builtin_amdgcn_s_sleep(PACE); SB(); }
    if (w == 0) STAMP(2);
    ctl_wait<1>(lds, C_Q, 2u);
    u32x4 qf[3];
#pragma unroll
    for (int t = 0; t < 3; ++t) qf[t] = *(const u32x4*)(lds + L_QS + t * 128 + c * 16);
    float s[8];
#pragma unroll
    for (int g = 0; g < 8; ++g) {
      float d0 = 0.f, d1 = 0.f, d2 = 0.f;
#pragma unroll
      for (int k = 0; k < 4; ++k) { d0 = dot2(K[g][0][k], qf[0][k], d0); d1 = dot2(K[g][1][k], qf[1][k], d1); d2 = dot2(K[g][2][k], qf[2][k], d2); }
      float d = (d0 + d1) + d2;
      asm volatile("" : "+v"(d));
      SB();
      if (g == 0) {
#pragma unroll
        for (int t = 0; t < 3; ++t) { V[NVPRE][t] = LDKV(rv, voff + t * 128, NVPRE * 24576); SB(); }
      }
      d += dpp<0xB1>(d); d += dpp<0x4E>(d); d += dpp<0x141>(d);
      s[g] = d * (0.07216878364870322f * 1.4426950408889634f);
    }
    float m = fmaxf(fmaxf(fmaxf(s[0], s[1]), fmaxf(s[2], s[3])), fmaxf(fmaxf(s[4], s[5]), fmaxf(s[6], s[7])));
    m = fmaxf(m, dpp<0x128>(m)); m = swap16_max(m); m = swap32_max(m);
    if (lane == 0) *(volatile LAS u32x2*)(lds + L_MISC + C_MX + w * 8) = u32x2{f2u(m), 1u};
    float l = 0.f;
    f32x2 acc[12];
#pragma unroll
    for (int i = 0; i < 12; ++i) acc[i] = f32x2{0.f, 0.f};
#pragma unroll
    for (int g = 0; g < 8; ++g) {
      if (g + NVPRE + 1 < 8) {
        waitvm<PVW>();
#pragma unroll
        for (int t = 0; t < 3; ++t) { V[g + NVPRE + 1][t] = LDKV(rv, voff + t * 128, (g + NVPRE + 1) * 24576); if (PACE2) __builtin_amdgcn_s_sleep(PACE2); SB(); }
        if (g + NVPRE + 1 == 7 && lane == 0) __hip_atomic_fetch_add((LAS unsigned*)(lds + L_MISC + C_VISS), 1u, __ATOMIC_RELAXED, __HIP_MEMORY_SCOPE_WORKGROUP);
      }
      if (g == 3) {
        u32x4 ma, mb, mc, md;
        for (unsigned spin = 0; spin < LSPIN; ++spin) {
          ma = *(volatile const LAS u32x4*)(lds + L_MISC + C_MX); mb = *(volatile const LAS u32x4*)(lds + L_MISC + C_MX + 16);
          mc = *(volatile const LAS u32x4*)(lds + L_MISC + C_MX + 32); md = *(volatile const LAS u32x4*)(lds + L_MISC + C_MX + 48);
          if ((ma[1] & ma[3] & mb[1] & mb[3] & mc[1] & mc[3] & md[1] & md[3]) != 0u) break;
          __builtin_amdgcn_s_sleep(1);
        }
        const float M = fmaxf(fmaxf(fmaxf(u2f(ma[0]), u2f(ma[2])), fmaxf(u2f(mb[0]), u2f(mb[2]))), fmaxf(fmaxf(u2f(mc[0]), u2f(mc[2])), fmaxf(u2f(md[0]), u2f(md[2]))));
        const float f = __builtin_amdgcn_exp2f(m - M);
        m = M; l *= f;
        const f32x2 f2 = {f, f};
#pragma unroll
        for (int i = 0; i < 12; ++i) acc[i] *= f2;
      }
      const float p = __builtin_amdgcn_exp2f(s[g] - m);
      l += p;
      const f32x2 p2 = {p, p};
#pragma unroll
      for (int t = 0; t < 3; ++t)
#pragma unroll
        for (int k = 0; k < 4; ++k) { const f32x2 v2 = {bf_lo(V[g][t][k]), bf_hi(V[g][t][k])}; acc[t * 4 + k] = __builtin_elementwise_fma(p2, v2, acc[t * 4 + k]); }
      asm volatile("" : "+v"(acc[0]), "+v"(acc[1]), "+v"(acc[2]), "+v"(acc[3]), "+v"(acc[4]), "+v"(acc[5]), "+v"(acc[6]), "+v"(acc[7]), "+v"(acc[8]), "+v"(acc[9]), "+v"(acc[10]), "+v"(acc[11]), "+v"(l));
      SB();
    }
    {
      float t1[12], t2[6];
#pragma unroll
      for (int a = 0; a < 12; ++a) t1[a] = swap32_add(acc[a >> 1][a & 1], acc[(a + 12) >> 1][a & 1]);
#pragma unroll
      for (int a = 0; a < 6; ++a) t2[a] = swap16_add(t1[a], t1[a + 6]);
#pragma unroll
      for (int a = 0; a < 6; ++a) t2[a] += dpp<0x128>(t2[a]);
      l += dpp<0x128>(l); l = swap16_add(l, l); l = swap32_add(l, l);
      const int rho = lane >> 4;
      if ((lane & 8) == 0) {
        float* red = (float*)(lds + L_RED) + w * 192;
#pragma unroll
        for (int i2 = 0; i2 < 3; ++i2) { const int a = 6 * rho + 2 * i2; *(f32x2*)(red + (a >> 3) * 64 + c * 8 + (a & 7)) = f32x2{t2[2 * i2], t2[2 * i2 + 1]}; }
      }
      if (lane == 0) *(float*)(lds + L_MISC + C_LS + w * 4) = l;
    }
    ctl_inc(lds, C_PV, lane);
    if (w == 0) STAMP(4);
    const unsigned tag = ctl_ld(lds, C_TAG);
    if (w >= 6) {
      const int u = (w - 6) * 64 + lane;
      const int toff = T1_OFF + ((h * 8 + j) * 104 + u) * 16;
      ctl_wait<0>(lds, C_PV, 8u);
      if (u < 96) {
        const float* red = (const float*)(lds + L_RED) + 2 * u;
        f32x2 o = *(const f32x2*)red;
#pragma unroll
        for (int ww = 1; ww < 8; ++ww) o += *(const f32x2*)(red + ww * 192);
        STSC(mkunit(o[0], o[1], tag), rs, toff);
      } else if (u < 104) {
        const f32x4 la = *(const f32x4*)(lds + L_MISC + C_LS), lb = *(const f32x4*)(lds + L_MISC + C_LS + 16);
        const float L = ((la[0] + la[1]) + (la[2] + la[3])) + ((lb[0] + lb[1]) + (lb[2] + lb[3]));
        STSC(mkunit(m, L, tag), rs, toff);
      }
      if (w == 6) {
        STAMP(11);
        outproj_tile(lds, rs, 4, lane, h, j, tag);
      } else {
        final_part(lds, rs, 0, lane, h, j, tag, out);
        if (wg == 0 && lane == 0) __hip_atomic_store((GAS unsigned*)ws, tag, __ATOMIC_RELAXED, __HIP_MEMORY_SCOPE_AGENT);
        STAMP(7);
      }
    } else {
      const int it = w * 64 + lane, bb = it / 96, p = it - bb * 96;
      const int o0 = T1_OFF + ((h * 8 + 2 * bb) * 104) * 16, o1 = o0 + 104 * 16;
      const int pa0 = o0 + p * 16, pm0 = o0 + 96 * 16, pa1 = o1 + p * 16, pm1 = o1 + 96 * 16;
      u32x4 R[4];
#pragma unroll
      for (int i = 0; i < 4; ++i) R[i] = u32x4{0u, 0u, 0u, 0u};
      ctl_wait<1>(lds, C_PV, 8u);
      {
        const int bbl = (w * 64) / 96, bbh = (w * 64 + 63) / 96;
        const int pj = (lane & 1) + 2 * ((lane & 2) ? bbh : bbl);
        const int so = T1_OFF + ((h * 8 + pj) * 104 + 96) * 16;
        const int nl = bbl == bbh ? 2 : 4;
        bool ok = lane >= nl;
        for (unsigned spin = 0; spin < SPIN_CAP; ++spin) {
          if (!ok) { const u32x4 sn = LDSC(rs, so); ok = okunit(sn, tag); }
          if (__popcll(__ballot(!ok)) <= T1DIR) break;
          __builtin_amdgcn_s_sleep(T1SLEEP);
        }
        bool ok0 = false, ok1 = false;
        for (unsigned spin = 0; spin < SPIN_CAP; ++spin) {
          if (!ok0) { R[0] = LDSC(rs, pa0); R[1] = LDSC(rs, pm0); }
          if (!ok1) { R[2] = LDSC(rs, pa1); R[3] = LDSC(rs, pm1); }
          ok0 = okunit(R[0], tag) && okunit(R[1], tag); ok1 = okunit(R[2], tag) && okunit(R[3], tag);
          if (__all(ok0 && ok1)) break;
          if (T1SLEEP2) __builtin_amdgcn_s_sleep(T1SLEEP2);
        }
      }
      const float M0 = u2f(R[1][0]), M1 = u2f(R[3][0]), M = fmaxf(M0, M1);
      const float e0 = __builtin_amdgcn_exp2f(M0 - M), e1 = __builtin_amdgcn_exp2f(M1 - M);
      const float inv = __builtin_amdgcn_rcpf(e0 * u2f(R[1][2]) + e1 * u2f(R[3][2]));
      const float f0 = e0 * inv, f1 = e1 * inv;
      *(unsigned*)(lds + L_OA + bb * 400 + p * 4) = cvtpk(f0 * u2f(R[0][0]) + f1 * u2f(R[2][0]), f0 * u2f(R[0][2]) + f1 * u2f(R[2][2]));
      ctl_inc(lds, C_OA, lane);
      if (w == 0) STAMP(12);
      if (w >= 4) final_part(lds, rs, w - 3, lane, h, j, tag, out);
    }
    STAMP_FLUSH();
  } else {
    __builtin_amdgcn_s_setprio(3);
    const int hw = wave, r = lane >> 3, c = lane & 7;
    const unsigned tag = __hip_atomic_load((GAS unsigned*)ws, __ATOMIC_RELAXED, __HIP_MEMORY_SCOPE_AGENT) + 1u;
    unsigned xp[3];
#pragma unroll
    for (int i = 0; i < 3; ++i) { int id = lane + 64 * i; id = id < 160 ? id : 159; const int bb = id / 40, kp = id - bb * 40;
      xp[i] = *(const GAS unsigned*)(x + bb * DM + 80 * j + 2 * kp); }
    SB();
    u32x4 W[10];
    {
      const int ce = (hw == 3 && c >= 6) ? 5 : c;
      const unsigned short* p0 = hw < 3 ? (wq + (size_t)(80 * j + 2 * r) * QD + h * DH + hw * 64 + ce * 8) : (w1 + (size_t)(80 * j + 2 * r) * DFF + h * 48 + ce * 8);
      const int rs1 = hw < 3 ? QD : DFF;
#pragma unroll
      for (int m = 0; m < 5; ++m)
#pragma unroll
        for (int sub = 0; sub < 2; ++sub) { W[2 * m + sub] = ldnt(p0 + (size_t)((16 * m + sub) * rs1)); SB(); }
    }
    if (hw == 0) ctl_st(lds, lane * 4, 0u);
    BARRIER();
#pragma unroll
    for (int i = 0; i < 3; ++i) { const int id = lane + 64 * i; if (id < 160) { const int bb = id / 40, kp = id - bb * 40; *(unsigned*)(lds + L_XS + hw * 640 + kp * 16 + bb * 4) = xp[i]; } }
    if (hw == 0 && lane == 0) ctl_st(lds, C_TAG, tag);
    {
      float acc[32];
#pragma unroll
      for (int i = 0; i < 32; ++i) acc[i] = 0.f;
#pragma unroll
      for (int m = 0; m < 5; ++m) {
        const u32x4 xq = *(const u32x4*)(lds + L_XS + hw * 640 + (8 * m + r) * 16);
#pragma unroll
        for (int d = 0; d < 4; ++d) {
          const unsigned lo = __builtin_amdgcn_perm(W[2 * m + 1][d], W[2 * m][d], 0x05040100u);
          const unsigned hi = __builtin_amdgcn_perm(W[2 * m + 1][d], W[2 * m][d], 0x07060302u);
#pragma unroll
          for (int bb = 0; bb < 4; ++bb) { acc[bb * 8 + 2 * d] = dot2(lo, xq[bb], acc[bb * 8 + 2 * d]); acc[bb * 8 + 2 * d + 1] = dot2(hi, xq[bb], acc[bb * 8 + 2 * d + 1]); }
        }
      }
      float t[16], u[8];
#pragma unroll
      for (int i = 0; i < 16; ++i) t[i] = swap32_add(acc[i], acc[i + 16]);
#pragma unroll
      for (int i = 0; i < 8; ++i) u[i] = swap16_add(t[i], t[i + 8]);
#pragma unroll
      for (int i = 0; i < 8; ++i) u[i] += dpp<0x128>(u[i]);
      const int bb = lane >> 4, hi4 = (lane >> 3) & 1;
      const float v0 = hi4 ? u[4] : u[0], v1 = hi4 ? u[5] : u[1], v2 = hi4 ? u[6] : u[2], v3 = hi4 ? u[7] : u[3];
      const int off = hw < 3 ? (T0Q_OFF + (((h * 4 + bb) * 8 + j) * 96 + hw * 32 + (lane & 15)) * 16)
                             : (T0H_OFF + (((h * 8 + j) * 4 + bb) * 32 + (lane & 15)) * 16);
      __builtin_amdgcn_raw_buffer_store_b128(mkunit(v0, v1, tag), rs, off, 0, T0AUX); __builtin_amdgcn_raw_buffer_store_b128(mkunit(v2, v3, tag), rs, off + 256, 0, T0AUX);
    }
    if (hw == 0) STAMP(9);
    if (hw == 0) {
      __builtin_amdgcn_s_sleep(T0DELAY);
      const int so = lane < 8 ? (T0Q_OFF + (((h * 4 + b) * 8 + lane) * 96 + 95) * 16) : (T0H_OFF + (((h * 8 + (lane & 7)) * 4 + 3) * 32 + 16) * 16);
      bool ok = lane >= 16;
      for (unsigned spin = 0; spin < SPIN_CAP; ++spin) {
        if (!ok) { const u32x4 sn = LDSC(rs, so); ok = okunit(sn, tag); }
        if (__all(ok)) break;
        __builtin_amdgcn_s_sleep(T0SLEEP);
      }
      if (lane == 0) ctl_st(lds, C_T0, 1u);
    } else if (hw == 2) {
      u32x4 G[10];
      const int l16 = lane < 16 ? lane + 64 : lane;
      G[0] = *(const GAS u32x4*)(gw + lane * 8); G[1] = *(const GAS u32x4*)(gw + l16 * 8);
#pragma unroll
      for (int bb = 0; bb < 4; ++bb) { G[2 + 2 * bb] = *(const GAS u32x4*)(x + bb * DM + lane * 8); G[3 + 2 * bb] = *(const GAS u32x4*)(x + bb * DM + l16 * 8); }
#pragma unroll
      for (int bb = 0; bb < 4; ++bb) {
        float s0 = 0.f, s1 = 0.f;
#pragma unroll
        for (int d = 0; d < 4; ++d) {
          s0 += rbf(bf_lo(G[2 + 2 * bb][d]) * bf_lo(G[0][d])); s0 += rbf(bf_hi(G[2 + 2 * bb][d]) * bf_hi(G[0][d]));
          s1 += rbf(bf_lo(G[3 + 2 * bb][d]) * bf_lo(G[1][d])); s1 += rbf(bf_hi(G[3 + 2 * bb][d]) * bf_hi(G[1][d]));
        }
        float t = s0 + (lane < 16 ? s1 : 0.f);
        t += dpp<0xB1>(t); t += dpp<0x4E>(t); t += dpp<0x141>(t); t += dpp<0x140>(t);
        t = swap16_add(t, t); t = swap32_add(t, t);
        t = rbf(t);
        const float g = rbf(1.f / (1.f + __expf(-t)));
        if (lane == 0) { *(float*)(lds + L_MISC + C_G + bb * 4) = g; *(float*)(lds + L_MISC + C_OMG + bb * 4) = rbf(1.f - g); }
      }
    } else if (hw == 3) {
      u32x4 W2[8];
      const int l60 = lane < 60 ? lane : 59, rb = l60 / 10, cq = l60 - rb * 10;
      const unsigned short* p2 = w2 + (size_t)(48 * h + 8 * rb) * DM + 80 * j + cq * 8;
#pragma unroll
      for (int i = 0; i < 8; ++i) W2[i] = ldnt(p2 + i * DM);
      if (lane < 60) {
        const int ks = rb >> 2, kg = rb & 3, nt = cq >> 1;
        const int dst = L_W2B + ((ks * 5 + nt) * 64 + kg * 16 + (cq & 1) * 8 + (cq & 7)) * 16;
#pragma unroll
        for (int cc = 0; cc < 8; ++cc) {
          const unsigned sel = (cc & 1) ? 0x07060302u : 0x05040100u;
          u32x4 o;
#pragma unroll
          for (int kk = 0; kk < 4; ++kk) o[kk] = __builtin_amdgcn_perm(W2[2 * kk + 1][cc >> 1], W2[2 * kk][cc >> 1], sel);
          *(u32x4*)(lds + (dst ^ (cc * 16))) = o;
        }
      }
#pragma unroll
      for (int i = 0; i < 3; ++i) { const int id = lane + 64 * i; if (id < 160) *(u32x4*)(lds + L_W2B + ((5 + id / 32) * 64 + 32 + (id & 31)) * 16) = u32x4{0u, 0u, 0u, 0u}; }
      if (lane < 32) *(unsigned*)(lds + L_HA + (lane >> 3) * 144 + (24 + (lane & 7)) * 4) = 0u;
    }
    ctl_wait<2>(lds, C_T0, 1u);
    {
      const int gi = (hw & 1) * 64 + lane; const bool live = gi < 96; const int gl = live ? gi : 95;
      const int hb = gl / 24, hq = gl - hb * 24, hsel = hq / 12, q2 = hq - hsel * 12, h4 = q2 / 6, hc = q2 - h4 * 6;
      const int off0 = hw < 2 ? (T0Q_OFF + ((h * 4 + b) * 8 * 96 + gl) * 16) : (T0H_OFF + ((h * 8 * 4 + hb) * 32 + hsel * 16 + h4 * 8 + hc) * 16);
      const int jstr = hw < 2 ? 1536 : 2048;
      u32x4 U[8];
      for (unsigned spin = 0; spin < SPIN_CAP; ++spin) {
        bool ok = true;
#pragma unroll
        for (int jj = 0; jj < 8; ++jj) U[jj] = LDSC(rs, off0 + jj * jstr);
#pragma unroll
        for (int jj = 0; jj < 8; ++jj) ok = ok && okunit(U[jj], tag);
        if (__all(ok)) break;
        __builtin_amdgcn_s_sleep(T0SLEEP);
      }
      float v0 = 0.f, v1 = 0.f;
#pragma unroll
      for (int jj = 0; jj < 8; ++jj) { v0 += u2f(U[jj][0]); v1 += u2f(U[jj][2]); }
      if (hw < 2) {
        const int blk = gl >> 5, pos = gl & 31, sel = pos >> 4, p4 = (pos >> 3) & 1, cc = pos & 7;
        if (live) *(unsigned*)(lds + L_QS + (blk * 32 + cc * 4 + p4 * 2 + sel) * 4) = cvtpk(v0, v1);
        ctl_inc(lds, C_Q, lane);
        if (hw == 0) STAMP(1);
      } else {
        if (live) *(unsigned*)(lds + L_HA + hb * 144 + (hc * 4 + h4 * 2 + hsel) * 4) = cvtpk(fmaxf(v0, 0.f), fmaxf(v1, 0.f));
        ctl_inc(lds, C_FFN, lane);
      }
    }
    ctl_wait<4>(lds, C_VISS, WO_WAIT);
    if (hw == 0) STAMP(14);
    {
      u32x4 WO[8];
      const int l60 = lane < 60 ? lane : 59, rbl = l60 / 10, cq = l60 - rbl * 10;
      const unsigned short* po = wo + (size_t)(192 * h + 8 * (6 * hw + rbl)) * DM + 80 * j + cq * 8;
#pragma unroll
      for (int i = 0; i < 8; ++i) { WO[i] = ldnt(po + i * DM); SB(); }
      if (lane < 60) {
        const int rb = 6 * hw + rbl, ks = rb >> 2, kg = rb & 3, nt = cq >> 1;
        const int dst = L_WB + ((ks * 5 + nt) * 64 + kg * 16 + (cq & 1) * 8 + (cq & 7)) * 16;
#pragma unroll
        for (int cc = 0; cc < 8; ++cc) {
          const unsigned sel = (cc & 1) ? 0x07060302u : 0x05040100u;
          u32x4 o;
#pragma unroll
          for (int kk = 0; kk < 4; ++kk) o[kk] = __builtin_amdgcn_perm(WO[2 * kk + 1][cc >> 1], WO[2 * kk][cc >> 1], sel);
          *(u32x4*)(lds + (dst ^ (cc * 16))) = o;
        }
      }
    }
    ctl_inc(lds, C_TILE, lane);
    if (hw == 0) STAMP(13);
    outproj_tile(lds, rs, hw, lane, h, j, tag);
    if (hw == 0) STAMP(6);
    STAMP_FLUSH();
  }
}

extern "C" void kernel_launch(void* const* d_in, const int* in_sizes, int n_in, void* d_out, int out_size, void* d_ws, size_t ws_size, hipStream_t stream) {
  fused_kernel<<<256, NTHR, 0, stream>>>((const unsigned short*)d_in[0], (const unsigned short*)d_in[1], (const unsigned short*)d_in[2],
      (const unsigned short*)d_in[3], (const unsigned short*)d_in[4], (const unsigned short*)d_in[6], (const unsigned short*)d_in[7],
      (const unsigned short*)d_in[8], (unsigned short*)d_out, (unsigned char*)d_ws
      );
}
```

```cpp
#include <hip/hip_runtime.h>
#include <hip/hip_bf16.h>
#include <stdint.h>

typedef unsigned u32x4 __attribute__((ext_vector_type(4)));
typedef unsigned u32x2 __attribute__((ext_vector_type(2)));
typedef float f32x4 __attribute__((ext_vector_type(4)));
typedef float f32x2 __attribute__((ext_vector_type(2)));
typedef short bf16x8 __attribute__((ext_vector_type(8)));
typedef __bf16 bf16x2_t __attribute__((ext_vector_type(2)));
#define GAS __attribute__((address_space(1)))
#define LAS __attribute__((address_space(3)))

#define DEPTH 6
#define WO_WAIT 8
#ifndef NVPRE
#define NVPRE 4
#endif
#define PVW 4
#define T0DELAY 24
#define T0SLEEP 12
#define T1POL 2
#define T2POL 1
#define T1SLEEP 4
#define T2SLEEP 4
#define LDAUX 16
#define STAUX 16
#define T0POL 1
#define T0AUX 16
#define PACE 11
#define PACE2 0
#define T1DIR 1
#define T1SLEEP2 1
#define ARGTOP 1
#define WOSRC C_VISS
#define WOSL 4
#define TOUCH 0
#define TSTEP 4096
#define PACEN 0
#define PACEI 11
#define POLLGAP 8
constexpr int DM = 640, NH = 32, SK = 4096, DH = 192, WIN = 1024, DFF = 1536, QD = NH * DH;
constexpr int NTHR = 768;
constexpr int T0F_OFF = 4096;
constexpr int T0Q_OFF = T0F_OFF + 32 * 8 * 128;
constexpr int T0H_OFF = T0Q_OFF + 32 * 24576;
constexpr int T1_OFF = T0H_OFF + 32 * 8 * 768;
constexpr int WS_USED = T1_OFF + 32 * 8 * 104 * 16;
constexpr unsigned SPIN_CAP = 1u << 15;
constexpr unsigned LSPIN = 1u << 22;
constexpr int L_XS = 0;
constexpr int L_QS = 2560;
constexpr int L_MISC = 2944;
constexpr int L_HA = 3200;
constexpr int L_OA = 3776;
constexpr int L_RED = 5376;
constexpr int L_WB = 11520;
constexpr int L_W2B = 42240;
constexpr int L_END = 52480;
static_assert(L_WB % 128 == 0 && L_W2B % 128 == 0, "swizzle");
constexpr int C_TAG = 0, C_VISS = 4, C_Q = 8, C_PV = 12, C_TILE = 16, C_FFN = 20, C_OA = 24, C_T0 = 28, C_DR = 32, C_LS = 96, C_G = 128, C_OMG = 144, C_MX = 160;

#define STAMP(i) do {} while (0)
#define STAMP_FLUSH() do {} while (0)

__device__ __forceinline__ float bf_lo(unsigned u) { return __builtin_bit_cast(float, u << 16); }
__device__ __forceinline__ float bf_hi(unsigned u) { return __builtin_bit_cast(float, u & 0xffff0000u); }
__device__ __forceinline__ unsigned cvtpk(float lo, float hi) {
  unsigned r; asm("v_cvt_pk_bf16_f32 %0, %1, %2" : "=v"(r) : "v"(lo), "v"(hi)); return r;
}
__device__ __forceinline__ float rbf(float f) { return bf_lo(cvtpk(f, f)); }
__device__ __forceinline__ float dot2(unsigned a, unsigned b, float acc) {
  return __builtin_amdgcn_fdot2_f32_bf16(__builtin_bit_cast(bf16x2_t, a), __builtin_bit_cast(bf16x2_t, b), acc, false);
}
template <int CTRL> __device__ __forceinline__ float dpp(float x) {
  return __builtin_bit_cast(float, __builtin_amdgcn_mov_dpp(__builtin_bit_cast(int, x), CTRL, 0xf, 0xf, true));
}
__device__ __forceinline__ float u2f(unsigned u) { return __builtin_bit_cast(float, u); }
__device__ __forceinline__ unsigned f2u(float f) { return __builtin_bit_cast(unsigned, f); }
__device__ __forceinline__ float swap32_add(float a, float b) {
  auto s = __builtin_amdgcn_permlane32_swap(f2u(a), f2u(b), false, false); return u2f(s[0]) + u2f(s[1]);
}
__device__ __forceinline__ float swap16_add(float a, float b) {
  auto s = __builtin_amdgcn_permlane16_swap(f2u(a), f2u(b), false, false); return u2f(s[0]) + u2f(s[1]);
}
__device__ __forceinline__ float swap32_max(float a) {
  auto s = __builtin_amdgcn_permlane32_swap(f2u(a), f2u(a), false, false); return fmaxf(u2f(s[0]), u2f(s[1]));
}
__device__ __forceinline__ float swap16_max(float a) {
  auto s = __builtin_amdgcn_permlane16_swap(f2u(a), f2u(a), false, false); return fmaxf(u2f(s[0]), u2f(s[1]));
}
#define SB() __builtin_amdgcn_sched_barrier(0)
#define BARRIER() asm volatile("s_waitcnt lgkmcnt(0)\n\ts_barrier" ::: "memory")
#define LGKM0() asm volatile("s_waitcnt lgkmcnt(0)" ::: "memory")
#define CBAR() asm volatile("" ::: "memory")
template <int N> __device__ __forceinline__ void waitvm() { asm volatile("s_waitcnt vmcnt(%0)" ::"n"(N)); }
#define LDKV(rs, vo, so) __builtin_amdgcn_raw_buffer_load_b128(rs, vo, so, 2)
__device__ __forceinline__ int opq(int v) { asm volatile("" : "+v"(v)); return v; }
#define LDSC(rs, vo) __builtin_amdgcn_raw_buffer_load_b128(rs, opq(vo), 0, LDAUX)
#define STSC(v, rs, vo) __builtin_amdgcn_raw_buffer_store_b128(v, rs, vo, 0, STAUX)
__device__ __forceinline__ u32x4 ldnt(const unsigned short* p) { return __builtin_nontemporal_load((const GAS u32x4*)p); }
__device__ __forceinline__ u32x4 mkunit(float a, float b, unsigned tag) { return u32x4{f2u(a), tag, f2u(b), tag}; }
__device__ __forceinline__ bool okunit(u32x4 u, unsigned tag) { return u[1] == tag && u[3] == tag; }
__device__ __forceinline__ unsigned ctl_ld(const unsigned char* lds, int off) { return *(volatile const LAS unsigned*)(lds + L_MISC + off); }
__device__ __forceinline__ void ctl_st(unsigned char* lds, int off, unsigned v) { *(volatile LAS unsigned*)(lds + L_MISC + off) = v; }
__device__ __forceinline__ void ctl_inc(unsigned char* lds, int off, int lane) {
  LGKM0();
  if (lane == 0) __hip_atomic_fetch_add((LAS unsigned*)(lds + L_MISC + off), 1u, __ATOMIC_RELAXED, __HIP_MEMORY_SCOPE_WORKGROUP);
}
template <int SLEEP> __device__ __forceinline__ void ctl_wait(const unsigned char* lds, int off, unsigned want) {
  for (unsigned spin = 0; spin < LSPIN; ++spin) { if (ctl_ld(lds, off) >= want) break; if (SLEEP) __builtin_amdgcn_s_sleep(SLEEP); }
  CBAR();
}

__device__ __forceinline__ void outproj_tile(unsigned char* lds, __amdgpu_buffer_rsrc_t rs, int nt, int lane, int h, int j, unsigned tag) {
  ctl_wait<1>(lds, C_TILE, 4u);
  ctl_wait<1>(lds, C_FFN, 2u);
  const int slot = (lane & 56) | ((lane & 7) ^ ((2 * nt + ((lane >> 3) & 1)) & 7));
  bf16x8 bf[6], haf[2], hbf[2], af[6];
#pragma unroll
  for (int ks = 0; ks < 6; ++ks) bf[ks] = *(const bf16x8*)(lds + L_WB + ((ks * 5 + nt) * 64 + slot) * 16);
#pragma unroll
  for (int ks = 0; ks < 2; ++ks) {
    haf[ks] = *(const bf16x8*)(lds + L_HA + (lane & 3) * 144 + ks * 64 + (lane >> 4) * 16);
    hbf[ks] = *(const bf16x8*)(lds + L_W2B + ((ks * 5 + nt) * 64 + slot) * 16);
  }
  const f32x4 ga = *(const f32x4*)(lds + L_MISC + C_G), gb = *(const f32x4*)(lds + L_MISC + C_OMG);
  f32x4 ab = {0.f, 0.f, 0.f, 0.f};
#pragma unroll
  for (int ks = 0; ks < 2; ++ks) ab = __builtin_amdgcn_mfma_f32_16x16x32_bf16(haf[ks], hbf[ks], ab, 0, 0, 0);
  ab *= gb;
  const int off = T0Q_OFF + h * 24576 + j * 2560 + (16 * nt + lane) * 16;
  const int aoff = L_OA + (lane & 3) * 400 + (lane >> 4) * 16;
  asm volatile("" : "+v"(ab), "+v"(bf[0]), "+v"(bf[1]), "+v"(bf[2]), "+v"(bf[3]), "+v"(bf[4]), "+v"(bf[5]));
  ctl_wait<0>(lds, C_OA, 6u);
#pragma unroll
  for (int ks = 0; ks < 6; ++ks) af[ks] = *(const bf16x8*)(lds + aoff + ks * 64);
  f32x4 aa = {0.f, 0.f, 0.f, 0.f}, aa2 = {0.f, 0.f, 0.f, 0.f};
#pragma unroll
  for (int ks = 0; ks < 3; ++ks) { aa = __builtin_amdgcn_mfma_f32_16x16x32_bf16(af[ks], bf[ks], aa, 0, 0, 0); aa2 = __builtin_amdgcn_mfma_f32_16x16x32_bf16(af[ks + 3], bf[ks + 3], aa2, 0, 0, 0); }
  if (lane < 16) {
    const f32x4 v = ga * (aa + aa2) + ab;
    STSC(mkunit(v[0], v[1], tag), rs, off); STSC(mkunit(v[2], v[3], tag), rs, off + 80 * 16);
  }
}

__device__ __forceinline__ void final_part(unsigned char* lds, __amdgpu_buffer_rsrc_t rs, int i, int lane, int h, int j, unsigned tag, unsigned short* out) {
  const int p = 2 * i + (lane >> 5);
  const int o0 = T0Q_OFF + (lane & 31) * 24576 + j * 2560 + (5 * h + p) * 16;
  const int u5 = 5 * h + p, bp = u5 >= 80 ? 1 : 0, cc = u5 - bp * 80;
  unsigned short* dst = out + (2 * bp) * DM + 80 * j + cc;
  ctl_wait<1>(lds, C_OA, 6u);
  u32x4 R = {0u, 0u, 0u, 0u};
  bool done = p >= 5;
  for (unsigned spin = 0; spin < SPIN_CAP; ++spin) {
    if (!done) { R = LDSC(rs, o0); done = okunit(R, tag); }
    if (__all(done)) break;
    __builtin_amdgcn_s_sleep(T2SLEEP);
  }
  float v0 = u2f(R[0]), v1 = u2f(R[2]);
  v0 += dpp<0xB1>(v0); v1 += dpp<0xB1>(v1); v0 += dpp<0x4E>(v0); v1 += dpp<0x4E>(v1);
  v0 += dpp<0x141>(v0); v1 += dpp<0x141>(v1); v0 += dpp<0x140>(v0); v1 += dpp<0x140>(v1);
  const float t = swap16_add(v0, v1);
  const unsigned pk = cvtpk(t, t);
  if ((lane & 15) == 0 && p < 5) dst[(lane & 16) ? DM : 0] = (unsigned short)(pk & 0xffffu);
}

__global__ void __launch_bounds__(NTHR, 3)
fused_kernel(const unsigned short* __restrict__ x, const unsigned short* __restrict__ kc, const unsigned short* __restrict__ vc,
             const unsigned short* __restrict__ wq, const unsigned short* __restrict__ wo, const unsigned short* __restrict__ w1,
             const unsigned short* __restrict__ w2, const unsigned short* __restrict__ gw, unsigned short* __restrict__ out,
             unsigned char* ws
             ) {
  __shared__ __attribute__((aligned(128))) unsigned char lds[L_END];
  asm volatile("" :: "s"(x), "s"(kc), "s"(vc), "s"(wq), "s"(wo), "s"(w1), "s"(w2), "s"(gw), "s"(out), "s"(ws));
  const int wg = blockIdx.x;
  const int xcd = wg & 7, ii = wg >> 3, h = xcd * 4 + (ii >> 3), j = ii & 7, b = j >> 1, half = j & 1;
  const int tid = threadIdx.x, lane = tid & 63, wave = __builtin_amdgcn_readfirstlane(tid >> 6);
  const __amdgpu_buffer_rsrc_t rs = __builtin_amdgcn_make_buffer_rsrc((void*)ws, 0, WS_USED, 0x00020000);

  if (wave >= 4) {
    const int w = wave - 4, r = lane >> 3, c = lane & 7;
    if (w == 0) STAMP(0);
    const size_t base = ((size_t)(b * NH + h) * SK + (SK - WIN) + half * 512) * DH * 2;
    const __amdgpu_buffer_rsrc_t rk = __builtin_amdgcn_make_buffer_rsrc((void*)((const char*)kc + base), 0, 512 * 384, 0x00020000);
    const __amdgpu_buffer_rsrc_t rv = __builtin_amdgcn_make_buffer_rsrc((void*)((const char*)vc + base), 0, 512 * 384, 0x00020000);
    const int voff = w * 3072 + r * 384 + c * 16;
    u32x4 K[8][3], V[8][3];
#pragma unroll
    for (int g = 0; g < 8; ++g)
#pragma unroll
      for (int t = 0; t < 3; ++t) {
        if (g * 3 + t == DEPTH) { BARRIER(); }
        if (g * 3 + t >= DEPTH) waitvm<DEPTH - 1>();
        K[g][t] = LDKV(rk, voff + t * 128, g * 24576); if (g * 3 + t < PACEN) __builtin_amdgcn_s_sleep(PACEI); else if (PACE) __builtin_amdgcn_s_sleep(PACE); SB();
      }
#pragma unroll
    for (int g = 0; g < NVPRE; ++g)
#pragma unroll
      for (int t = 0; t < 3; ++t) { waitvm<DEPTH - 1>(); V[g][t] = LDKV(rv, voff + t * 128, g * 24576); if (PACE) __builtin_amdgcn_s_sleep(PACE); SB(); }
    if (w == 0) STAMP(2);
    ctl_wait<1>(lds, C_Q, 1u);
    u32x4 qf[3];
#pragma unroll
    for (int t = 0; t < 3; ++t) qf[t] = *(const u32x4*)(lds + L_QS + t * 128 + c * 16);
    float s[8];
#pragma unroll
    for (int g = 0; g < 8; ++g) {
      float d0 = 0.f, d1 = 0.f, d2 = 0.f;
#pragma unroll
      for (int k = 0; k < 4; ++k) { d0 = dot2(K[g][0][k], qf[0][k], d0); d1 = dot2(K[g][1][k], qf[1][k], d1); d2 = dot2(K[g][2][k], qf[2][k], d2); }
      float d = (d0 + d1) + d2;
      asm volatile("" : "+v"(d));
      SB();
      if (g == 0) {
#pragma unroll
        for (int t = 0; t < 3; ++t) { V[NVPRE][t] = LDKV(rv, voff + t * 128, NVPRE * 24576); SB(); }
      }
      d += dpp<0xB1>(d); d += dpp<0x4E>(d); d += dpp<0x141>(d);
      s[g] = d * (0.07216878364870322f * 1.4426950408889634f);
    }
    float m = fmaxf(fmaxf(fmaxf(s[0], s[1]), fmaxf(s[2], s[3])), fmaxf(fmaxf(s[4], s[5]), fmaxf(s[6], s[7])));
    m = fmaxf(m, dpp<0x128>(m)); m = swap16_max(m); m = swap32_max(m);
    if (lane == 0) *(volatile LAS u32x2*)(lds + L_MISC + C_MX + w * 8) = u32x2{f2u(m), 1u};
    float l = 0.f;
    f32x2 acc[12];
#pragma unroll
    for (int i = 0; i < 12; ++i) acc[i] = f32x2{0.f, 0.f};
#pragma unroll
    for (int g = 0; g < 8; ++g) {
      if (g + NVPRE + 1 < 8) {
        waitvm<PVW>();
#pragma unroll
        for (int t = 0; t < 3; ++t) { V[g + NVPRE + 1][t] = LDKV(rv, voff + t * 128, (g + NVPRE + 1) * 24576); if (PACE2) __builtin_amdgcn_s_sleep(PACE2); SB(); }
        if (g + NVPRE + 1 == 7 && lane == 0) __hip_atomic_fetch_add((LAS unsigned*)(lds + L_MISC + C_VISS), 1u, __ATOMIC_RELAXED, __HIP_MEMORY_SCOPE_WORKGROUP);
      }
      if (g == 3) {
        u32x4 ma, mb, mc, md;
        for (unsigned spin = 0; spin < LSPIN; ++spin) {
          ma = *(volatile const LAS u32x4*)(lds + L_MISC + C_MX); mb = *(volatile const LAS u32x4*)(lds + L_MISC + C_MX + 16);
          mc = *(volatile const LAS u32x4*)(lds + L_MISC + C_MX + 32); md = *(volatile const LAS u32x4*)(lds + L_MISC + C_MX + 48);
          if ((ma[1] & ma[3] & mb[1] & mb[3] & mc[1] & mc[3] & md[1] & md[3]) != 0u) break;
          __builtin_amdgcn_s_sleep(1);
        }
        const float M = fmaxf(fmaxf(fmaxf(u2f(ma[0]), u2f(ma[2])), fmaxf(u2f(mb[0]), u2f(mb[2]))), fmaxf(fmaxf(u2f(mc[0]), u2f(mc[2])), fmaxf(u2f(md[0]), u2f(md[2]))));
        const float f = __builtin_amdgcn_exp2f(m - M);
        m = M; l *= f;
        const f32x2 f2 = {f, f};
#pragma unroll
        for (int i = 0; i < 12; ++i) acc[i] *= f2;
      }
      const float p = __builtin_amdgcn_exp2f(s[g] - m);
      l += p;
      const f32x2 p2 = {p, p};
#pragma unroll
      for (int t = 0; t < 3; ++t)
#pragma unroll
        for (int k = 0; k < 4; ++k) { const f32x2 v2 = {bf_lo(V[g][t][k]), bf_hi(V[g][t][k])}; acc[t * 4 + k] = __builtin_elementwise_fma(p2, v2, acc[t * 4 + k]); }
      asm volatile("" : "+v"(acc[0]), "+v"(acc[1]), "+v"(acc[2]), "+v"(acc[3]), "+v"(acc[4]), "+v"(acc[5]), "+v"(acc[6]), "+v"(acc[7]), "+v"(acc[8]), "+v"(acc[9]), "+v"(acc[10]), "+v"(acc[11]), "+v"(l));
      SB();
    }
    {
      float t1[12], t2[6];
#pragma unroll
      for (int a = 0; a < 12; ++a) t1[a] = swap32_add(acc[a >> 1][a & 1], acc[(a + 12) >> 1][a & 1]);
#pragma unroll
      for (int a = 0; a < 6; ++a) t2[a] = swap16_add(t1[a], t1[a + 6]);
#pragma unroll
      for (int a = 0; a < 6; ++a) t2[a] += dpp<0x128>(t2[a]);
      l += dpp<0x128>(l); l = swap16_add(l, l); l = swap32_add(l, l);
      const int rho = lane >> 4;
      if ((lane & 8) == 0) {
        float* red = (float*)(lds + L_RED) + w * 192;
#pragma unroll
        for (int i2 = 0; i2 < 3; ++i2) { const int a = 6 * rho + 2 * i2; *(f32x2*)(red + (a >> 3) * 64 + c * 8 + (a & 7)) = f32x2{t2[2 * i2], t2[2 * i2 + 1]}; }
      }
      if (lane == 0) *(float*)(lds + L_MISC + C_LS + w * 4) = l;
    }
    ctl_inc(lds, C_PV, lane);
    if (w == 0) STAMP(4);
    const unsigned tag = ctl_ld(lds, C_TAG);
    if (w >= 6) {
      const int u = (w - 6) * 64 + lane;
      const int toff = T1_OFF + ((h * 8 + j) * 104 + u) * 16;
      ctl_wait<0>(lds, C_PV, 8u);
      if (u < 96) {
        const float* red = (const float*)(lds + L_RED) + 2 * u;
        f32x2 o = *(const f32x2*)red;
#pragma unroll
        for (int ww = 1; ww < 8; ++ww) o += *(const f32x2*)(red + ww * 192);
        STSC(mkunit(o[0], o[1], tag), rs, toff);
      } else if (u < 104) {
        const f32x4 la = *(const f32x4*)(lds + L_MISC + C_LS), lb = *(const f32x4*)(lds + L_MISC + C_LS + 16);
        const float L = ((la[0] + la[1]) + (la[2] + la[3])) + ((lb[0] + lb[1]) + (lb[2] + lb[3]));
        STSC(mkunit(m, L, tag), rs, toff);
      }
      if (w == 6) {
        STAMP(11);
        outproj_tile(lds, rs, 4, lane, h, j, tag);
      } else {
        final_part(lds, rs, 0, lane, h, j, tag, out);
        if (wg == 0 && lane == 0) __hip_atomic_store((GAS unsigned*)ws, tag, __ATOMIC_RELAXED, __HIP_MEMORY_SCOPE_AGENT);
        STAMP(7);
      }
    } else {
      const int it = w * 64 + lane, bb = it / 96, p = it - bb * 96;
      const int o0 = T1_OFF + ((h * 8 + 2 * bb) * 104) * 16, o1 = o0 + 104 * 16;
      const int pa0 = o0 + p * 16, pm0 = o0 + 96 * 16, pa1 = o1 + p * 16, pm1 = o1 + 96 * 16;
      u32x4 R[4];
#pragma unroll
      for (int i = 0; i < 4; ++i) R[i] = u32x4{0u, 0u, 0u, 0u};
      ctl_wait<1>(lds, C_PV, 8u);
      {
        const int bbl = (w * 64) / 96, bbh = (w * 64 + 63) / 96;
        const int pj = (lane & 1) + 2 * ((lane & 2) ? bbh : bbl);
        const int so = T1_OFF + ((h * 8 + pj) * 104 + 96) * 16;
        const int nl = bbl == bbh ? 2 : 4;
        bool ok = lane >= nl;
        for (unsigned spin = 0; spin < SPIN_CAP; ++spin) {
          if (!ok) { const u32x4 sn = LDSC(rs, so); ok = okunit(sn, tag); }
          if (__popcll(__ballot(!ok)) <= T1DIR) break;
          __builtin_amdgcn_s_sleep(T1SLEEP);
        }
        bool ok0 = false, ok1 = false;
        for (unsigned spin = 0; spin < SPIN_CAP; ++spin) {
          if (!ok0) { R[0] = LDSC(rs, pa0); R[1] = LDSC(rs, pm0); }
          if (!ok1) { R[2] = LDSC(rs, pa1); R[3] = LDSC(rs, pm1); }
          ok0 = okunit(R[0], tag) && okunit(R[1], tag); ok1 = okunit(R[2], tag) && okunit(R[3], tag);
          if (__all(ok0 && ok1)) break;
          if (T1SLEEP2) __builtin_amdgcn_s_sleep(T1SLEEP2);
        }
      }
      const float M0 = u2f(R[1][0]), M1 = u2f(R[3][0]), M = fmaxf(M0, M1);
      const float e0 = __builtin_amdgcn_exp2f(M0 - M), e1 = __builtin_amdgcn_exp2f(M1 - M);
      const float inv = __builtin_amdgcn_rcpf(e0 * u2f(R[1][2]) + e1 * u2f(R[3][2]));
      const float f0 = e0 * inv, f1 = e1 * inv;
      *(unsigned*)(lds + L_OA + bb * 400 + p * 4) = cvtpk(f0 * u2f(R[0][0]) + f1 * u2f(R[2][0]), f0 * u2f(R[0][2]) + f1 * u2f(R[2][2]));
      ctl_inc(lds, C_OA, lane);
      if (w == 0) STAMP(12);
      if (w >= 4) final_part(lds, rs, w - 3, lane, h, j, tag, out);
    }
    STAMP_FLUSH();
  } else {
    __builtin_amdgcn_s_setprio(3);
    const int hw = wave, r = lane >> 3, c = lane & 7;
    const unsigned tag = __hip_atomic_load((GAS unsigned*)ws, __ATOMIC_RELAXED, __HIP_MEMORY_SCOPE_AGENT) + 1u;
    unsigned xp[3];
#pragma unroll
    for (int i = 0; i < 3; ++i) { int id = lane + 64 * i; id = id < 160 ? id : 159; const int bb = id / 40, kp = id - bb * 40;
      xp[i] = *(const GAS unsigned*)(x + bb * DM + 80 * j + 2 * kp); }
    SB();
    u32x4 W[10];
    {
      const int ce = (hw == 3 && c >= 6) ? 5 : c;
      const unsigned short* p0 = hw < 3 ? (wq + (size_t)(80 * j + 2 * r) * QD + h * DH + hw * 64 + ce * 8) : (w1 + (size_t)(80 * j + 2 * r) * DFF + h * 48 + ce * 8);
      const int rs1 = hw < 3 ? QD : DFF;
#pragma unroll
      for (int m = 0; m < 5; ++m)
#pragma unroll
        for (int sub = 0; sub < 2; ++sub) { W[2 * m + sub] = ldnt(p0 + (size_t)((16 * m + sub) * rs1)); SB(); }
    }
    if (hw == 0) ctl_st(lds, lane * 4, 0u);
    BARRIER();
#pragma unroll
    for (int i = 0; i < 3; ++i) { const int id = lane + 64 * i; if (id < 160) { const int bb = id / 40, kp = id - bb * 40; *(unsigned*)(lds + L_XS + hw * 640 + kp * 16 + bb * 4) = xp[i]; } }
    if (hw == 0 && lane == 0) ctl_st(lds, C_TAG, tag);
    {
      float acc[32];
#pragma unroll
      for (int i = 0; i < 32; ++i) acc[i] = 0.f;
#pragma unroll
      for (int m = 0; m < 5; ++m) {
        const u32x4 xq = *(const u32x4*)(lds + L_XS + hw * 640 + (8 * m + r) * 16);
#pragma unroll
        for (int d = 0; d < 4; ++d) {
          const unsigned lo = __builtin_amdgcn_perm(W[2 * m + 1][d], W[2 * m][d], 0x05040100u);
          const unsigned hi = __builtin_amdgcn_perm(W[2 * m + 1][d], W[2 * m][d], 0x07060302u);
#pragma unroll
          for (int bb = 0; bb < 4; ++bb) { acc[bb * 8 + 2 * d] = dot2(lo, xq[bb], acc[bb * 8 + 2 * d]); acc[bb * 8 + 2 * d + 1] = dot2(hi, xq[bb], acc[bb * 8 + 2 * d + 1]); }
        }
      }
      float t[16], u[8];
#pragma unroll
      for (int i = 0; i < 16; ++i) t[i] = swap32_add(acc[i], acc[i + 16]);
#pragma unroll
      for (int i = 0; i < 8; ++i) u[i] = swap16_add(t[i], t[i + 8]);
#pragma unroll
      for (int i = 0; i < 8; ++i) u[i] += dpp<0x128>(u[i]);
      const int bb = lane >> 4, hi4 = (lane >> 3) & 1;
      const f32x4 v = {hi4 ? u[4] : u[0], hi4 ? u[5] : u[1], hi4 ? u[6] : u[2], hi4 ? u[7] : u[3]};
      const int off = hw < 3 ? (T0Q_OFF + (((h * 4 + bb) * 8 + j) * 192 + hw * 64 + c * 8 + hi4 * 4) * 4)
                             : (T0H_OFF + (((h * 8 + j) * 4 + bb) * 48 + c * 8 + hi4 * 4) * 4);
      if (hw < 3 || c < 6) STSC(__builtin_bit_cast(u32x4, v), rs, off);
    }
    asm volatile("s_waitcnt vmcnt(0)" ::: "memory");
    {
      unsigned old4 = 0;
      if (lane == 0) old4 = __hip_atomic_fetch_add((LAS unsigned*)(lds + L_MISC + C_DR), 1u, __ATOMIC_RELAXED, __HIP_MEMORY_SCOPE_WORKGROUP);
      if (__builtin_amdgcn_readfirstlane(old4) == 3u && lane < 8) STSC((u32x4{tag, tag, tag, tag}), rs, T0F_OFF + (h * 8 + j) * 128 + lane * 16);
    }
    if (hw == 0) STAMP(9);
    if (hw == 0) {
      __builtin_amdgcn_s_sleep(T0DELAY);
      const int so = T0F_OFF + (h * 8 + (lane & 7)) * 128;
      bool ok = lane >= 8;
      for (unsigned spin = 0; spin < SPIN_CAP; ++spin) {
        if (!ok) { const u32x4 sn = LDSC(rs, so); ok = okunit(sn, tag); }
        if (__all(ok)) break;
        __builtin_amdgcn_s_sleep(T0SLEEP);
      }
      if (lane == 0) ctl_st(lds, C_T0, 1u);
    } else if (hw == 2) {
      u32x4 G[10];
      const int l16 = lane < 16 ? lane + 64 : lane;
      G[0] = *(const GAS u32x4*)(gw + lane * 8); G[1] = *(const GAS u32x4*)(gw + l16 * 8);
#pragma unroll
      for (int bb = 0; bb < 4; ++bb) { G[2 + 2 * bb] = *(const GAS u32x4*)(x + bb * DM + lane * 8); G[3 + 2 * bb] = *(const GAS u32x4*)(x + bb * DM + l16 * 8); }
#pragma unroll
      for (int bb = 0; bb < 4; ++bb) {
        float s0 = 0.f, s1 = 0.f;
#pragma unroll
        for (int d = 0; d < 4; ++d) {
          s0 += rbf(bf_lo(G[2 + 2 * bb][d]) * bf_lo(G[0][d])); s0 += rbf(bf_hi(G[2 + 2 * bb][d]) * bf_hi(G[0][d]));
          s1 += rbf(bf_lo(G[3 + 2 * bb][d]) * bf_lo(G[1][d])); s1 += rbf(bf_hi(G[3 + 2 * bb][d]) * bf_hi(G[1][d]));
        }
        float t = s0 + (lane < 16 ? s1 : 0.f);
        t += dpp<0xB1>(t); t += dpp<0x4E>(t); t += dpp<0x141>(t); t += dpp<0x140>(t);
        t = swap16_add(t, t); t = swap32_add(t, t);
        t = rbf(t);
        const float g = rbf(1.f / (1.f + __expf(-t)));
        if (lane == 0) { *(float*)(lds + L_MISC + C_G + bb * 4) = g; *(float*)(lds + L_MISC + C_OMG + bb * 4) = rbf(1.f - g); }
      }
    } else if (hw == 3) {
      u32x4 W2[8];
      const int l60 = lane < 60 ? lane : 59, rb = l60 / 10, cq = l60 - rb * 10;
      const unsigned short* p2 = w2 + (size_t)(48 * h + 8 * rb) * DM + 80 * j + cq * 8;
#pragma unroll
      for (int i = 0; i < 8; ++i) W2[i] = ldnt(p2 + i * DM);
      if (lane < 60) {
        const int ks = rb >> 2, kg = rb & 3, nt = cq >> 1;
        const int dst = L_W2B + ((ks * 5 + nt) * 64 + kg * 16 + (cq & 1) * 8 + (cq & 7)) * 16;
#pragma unroll
        for (int cc = 0; cc < 8; ++cc) {
          const unsigned sel = (cc & 1) ? 0x07060302u : 0x05040100u;
          u32x4 o;
#pragma unroll
          for (int kk = 0; kk < 4; ++kk) o[kk] = __builtin_amdgcn_perm(W2[2 * kk + 1][cc >> 1], W2[2 * kk][cc >> 1], sel);
          *(u32x4*)(lds + (dst ^ (cc * 16))) = o;
        }
      }
#pragma unroll
      for (int i = 0; i < 3; ++i) { const int id = lane + 64 * i; if (id < 160) *(u32x4*)(lds + L_W2B + ((5 + id / 32) * 64 + 32 + (id & 31)) * 16) = u32x4{0u, 0u, 0u, 0u}; }
      if (lane < 32) *(unsigned*)(lds + L_HA + (lane >> 3) * 144 + (24 + (lane & 7)) * 4) = 0u;
    }
    if (hw == 0 || hw == 2) {
      ctl_wait<2>(lds, C_T0, 1u);
      __builtin_amdgcn_fence(__ATOMIC_ACQUIRE, "wavefront");
      const int l48 = lane < 48 ? lane : 47;
      const int hb = l48 / 12, hq = l48 - hb * 12;
      const int off0 = hw == 0 ? (T0Q_OFF + ((h * 4 + b) * 8 * 192 + l48 * 4) * 4) : (T0H_OFF + ((h * 8 * 4 + hb) * 48 + hq * 4) * 4);
      u32x4 U[8];
#pragma unroll
      for (int jj = 0; jj < 8; ++jj) U[jj] = LDSC(rs, off0 + jj * 768);
      f32x4 v = __builtin_bit_cast(f32x4, U[0]);
#pragma unroll
      for (int jj = 1; jj < 8; ++jj) v += __builtin_bit_cast(f32x4, U[jj]);
      if (hw == 0) {
        if (lane < 48) *(u32x2*)(lds + L_QS + lane * 8) = u32x2{cvtpk(v[0], v[1]), cvtpk(v[2], v[3])};
        ctl_inc(lds, C_Q, lane);
        STAMP(1);
      } else {
        if (lane < 48) *(u32x2*)(lds + L_HA + hb * 144 + hq * 8) = u32x2{cvtpk(fmaxf(v[0], 0.f), fmaxf(v[1], 0.f)), cvtpk(fmaxf(v[2], 0.f), fmaxf(v[3], 0.f))};
      }
    }
    if (hw >= 2) ctl_inc(lds, C_FFN, lane);
    ctl_wait<WOSL>(lds, WOSRC, WO_WAIT);
    if (hw == 0) STAMP(14);
    {
      u32x4 WO[8];
      const int l60 = lane < 60 ? lane : 59, rbl = l60 / 10, cq = l60 - rbl * 10;
      const unsigned short* po = wo + (size_t)(192 * h + 8 * (6 * hw + rbl)) * DM + 80 * j + cq * 8;
#pragma unroll
      for (int i = 0; i < 8; ++i) { WO[i] = ldnt(po + i * DM); SB(); }
      if (lane < 60) {
        const int rb = 6 * hw + rbl, ks = rb >> 2, kg = rb & 3, nt = cq >> 1;
        const int dst = L_WB + ((ks * 5 + nt) * 64 + kg * 16 + (cq & 1) * 8 + (cq & 7)) * 16;
#pragma unroll
        for (int cc = 0; cc < 8; ++cc) {
          const unsigned sel = (cc & 1) ? 0x07060302u : 0x05040100u;
          u32x4 o;
#pragma unroll
          for (int kk = 0; kk < 4; ++kk) o[kk] = __builtin_amdgcn_perm(WO[2 * kk + 1][cc >> 1], WO[2 * kk][cc >> 1], sel);
          *(u32x4*)(lds + (dst ^ (cc * 16))) = o;
        }
      }
    }
    ctl_inc(lds, C_TILE, lane);
    if (hw == 0) STAMP(13);
    outproj_tile(lds, rs, hw, lane, h, j, tag);
    if (hw == 0) STAMP(6);
    STAMP_FLUSH();
  }
}

extern "C" void kernel_launch(void* const* d_in, const int* in_sizes, int n_in, void* d_out, int out_size, void* d_ws, size_t ws_size, hipStream_t stream) {
  fused_kernel<<<256, NTHR, 0, stream>>>((const unsigned short*)d_in[0], (const unsigned short*)d_in[1], (const unsigned short*)d_in[2],
      (const unsigned short*)d_in[3], (const unsigned short*)d_in[4], (const unsigned short*)d_in[6], (const unsigned short*)d_in[7],
      (const unsigned short*)d_in[8], (unsigned short*)d_out, (unsigned char*)d_ws
      );
}
```

```cpp
#include <hip/hip_runtime.h>
#include <hip/hip_bf16.h>
#include <stdint.h>

typedef unsigned u32x4 __attribute__((ext_vector_type(4)));
typedef unsigned u32x2 __attribute__((ext_vector_type(2)));
typedef float f32x4 __attribute__((ext_vector_type(4)));
typedef float f32x2 __attribute__((ext_vector_type(2)));
typedef short bf16x8 __attribute__((ext_vector_type(8)));
typedef __bf16 bf16x2_t __attribute__((ext_vector_type(2)));
#define GAS __attribute__((address_space(1)))
#define LAS __attribute__((address_space(3)))

#define DEPTH 6
#define WO_WAIT 8
#ifndef NVPRE
#define NVPRE 4
#endif
#define PVW 3
#define T0DELAY 40
#define T0SLEEP 12
#define T1POL 2
#define T2POL 1
#define T1SLEEP 4
#define T2SLEEP 4
#define LDAUX 16
#define STAUX 16
#define T0POL 1
#define T0AUX 16
#define PACE 11
#define PACE2 0
#define T1DIR 1
#define T1SLEEP2 1
#define ARGTOP 1
#define WOSRC C_VISS
#define WOSL 4
#define TOUCH 0
#define TSTEP 4096
#define POLLGAP 8
constexpr int DM = 640, NH = 32, SK = 4096, DH = 192, WIN = 1024, DFF = 1536, QD = NH * DH;
constexpr int NTHR = 768;
constexpr int T0Q_OFF = 4096;
constexpr int T0H_OFF = T0Q_OFF + 32 * 4 * 8 * 96 * 16;
constexpr int T1_OFF = T0H_OFF + 32 * 8 * 128 * 16;
constexpr int T2_OFF = T1_OFF + 32 * 8 * 104 * 16;
constexpr int WS_USED = T2_OFF + 8 * 32 * 160 * 16;
constexpr unsigned SPIN_CAP = 1u << 15;
constexpr unsigned LSPIN = 1u << 22;
constexpr int L_XS = 0;
constexpr int L_QS = 2560;
constexpr int L_MISC = 2944;
constexpr int L_HA = 3200;
constexpr int L_OA = 3776;
constexpr int L_RED = 5376;
constexpr int L_WB = 11520;
constexpr int L_W2B = 42240;
constexpr int L_END = 52480;
static_assert(L_WB % 128 == 0 && L_W2B % 128 == 0, "swizzle");
constexpr int C_TAG = 0, C_VISS = 4, C_Q = 8, C_PV = 12, C_TILE = 16, C_FFN = 20, C_OA = 24, C_T0 = 28, C_LS = 96, C_G = 128, C_OMG = 144, C_MX = 160;

#define STAMP(i) do {} while (0)
#define STAMP_FLUSH() do {} while (0)

__device__ __forceinline__ float bf_lo(unsigned u) { return __builtin_bit_cast(float, u << 16); }
__device__ __forceinline__ float bf_hi(unsigned u) { return __builtin_bit_cast(float, u & 0xffff0000u); }
__device__ __forceinline__ unsigned cvtpk(float lo, float hi) {
  unsigned r; asm("v_cvt_pk_bf16_f32 %0, %1, %2" : "=v"(r) : "v"(lo), "v"(hi)); return r;
}
__device__ __forceinline__ float rbf(float f) { return bf_lo(cvtpk(f, f)); }
__device__ __forceinline__ float dot2(unsigned a, unsigned b, float acc) {
  return __builtin_amdgcn_fdot2_f32_bf16(__builtin_bit_cast(bf16x2_t, a), __builtin_bit_cast(bf16x2_t, b), acc, false);
}
template <int CTRL> __device__ __forceinline__ float dpp(float x) {
  return __builtin_bit_cast(float, __builtin_amdgcn_mov_dpp(__builtin_bit_cast(int, x), CTRL, 0xf, 0xf, true));
}
__device__ __forceinline__ float u2f(unsigned u) { return __builtin_bit_cast(float, u); }
__device__ __forceinline__ unsigned f2u(float f) { return __builtin_bit_cast(unsigned, f); }
__device__ __forceinline__ float swap32_add(float a, float b) {
  auto s = __builtin_amdgcn_permlane32_swap(f2u(a), f2u(b), false, false); return u2f(s[0]) + u2f(s[1]);
}
__device__ __forceinline__ float swap16_add(float a, float b) {
  auto s = __builtin_amdgcn_permlane16_swap(f2u(a), f2u(b), false, false); return u2f(s[0]) + u2f(s[1]);
}
__device__ __forceinline__ float swap32_max(float a) {
  auto s = __builtin_amdgcn_permlane32_swap(f2u(a), f2u(a), false, false); return fmaxf(u2f(s[0]), u2f(s[1]));
}
__device__ __forceinline__ float swap16_max(float a) {
  auto s = __builtin_amdgcn_permlane16_swap(f2u(a), f2u(a), false, false); return fmaxf(u2f(s[0]), u2f(s[1]));
}
#define SB() __builtin_amdgcn_sched_barrier(0)
#define BARRIER() asm volatile("s_waitcnt lgkmcnt(0)\n\ts_barrier" ::: "memory")
#define LGKM0() asm volatile("s_waitcnt lgkmcnt(0)" ::: "memory")
#define CBAR() asm volatile("" ::: "memory")
template <int N> __device__ __forceinline__ void waitvm() { asm volatile("s_waitcnt vmcnt(%0)" ::"n"(N)); }
#define LDKV(rs, vo, so) __builtin_amdgcn_raw_buffer_load_b128(rs, vo, so, 2)
__device__ __forceinline__ int opq(int v) { asm volatile("" : "+v"(v)); return v; }
#define LDSC(rs, vo) __builtin_amdgcn_raw_buffer_load_b128(rs, opq(vo), 0, LDAUX)
#define STSC(v, rs, vo) __builtin_amdgcn_raw_buffer_store_b128(v, rs, vo, 0, STAUX)
__device__ __forceinline__ u32x4 ldnt(const unsigned short* p) { return __builtin_nontemporal_load((const GAS u32x4*)p); }
__device__ __forceinline__ u32x4 mkunit(float a, float b, unsigned tag) { return u32x4{f2u(a), tag, f2u(b), tag}; }
__device__ __forceinline__ bool okunit(u32x4 u, unsigned tag) { return u[1] == tag && u[3] == tag; }
__device__ __forceinline__ unsigned ctl_ld(const unsigned char* lds, int off) { return *(volatile const LAS unsigned*)(lds + L_MISC + off); }
__device__ __forceinline__ void ctl_st(unsigned char* lds, int off, unsigned v) { *(volatile LAS unsigned*)(lds + L_MISC + off) = v; }
__device__ __forceinline__ void ctl_inc(unsigned char* lds, int off, int lane) {
  LGKM0();
  if (lane == 0) __hip_atomic_fetch_add((LAS unsigned*)(lds + L_MISC + off), 1u, __ATOMIC_RELAXED, __HIP_MEMORY_SCOPE_WORKGROUP);
}
template <int SLEEP> __device__ __forceinline__ void ctl_wait(const unsigned char* lds, int off, unsigned want) {
  for (unsigned spin = 0; spin < LSPIN; ++spin) { if (ctl_ld(lds, off) >= want) break; if (SLEEP) __builtin_amdgcn_s_sleep(SLEEP); }
  CBAR();
}

__device__ __forceinline__ void outproj_tile(unsigned char* lds, __amdgpu_buffer_rsrc_t rs, int nt, int lane, int h, int j, unsigned tag) {
  ctl_wait<1>(lds, C_TILE, 4u);
  ctl_wait<1>(lds, C_FFN, 2u);
  const int slot = (lane & 56) | ((lane & 7) ^ ((2 * nt + ((lane >> 3) & 1)) & 7));
  bf16x8 bf[6], haf[2], hbf[2], af[6];
#pragma unroll
  for (int ks = 0; ks < 6; ++ks) bf[ks] = *(const bf16x8*)(lds + L_WB + ((ks * 5 + nt) * 64 + slot) * 16);
#pragma unroll
  for (int ks = 0; ks < 2; ++ks) {
    haf[ks] = *(const bf16x8*)(lds + L_HA + (lane & 3) * 144 + ks * 64 + (lane >> 4) * 16);
    hbf[ks] = *(const bf16x8*)(lds + L_W2B + ((ks * 5 + nt) * 64 + slot) * 16);
  }
  const f32x4 ga = *(const f32x4*)(lds + L_MISC + C_G), gb = *(const f32x4*)(lds + L_MISC + C_OMG);
  f32x4 ab = {0.f, 0.f, 0.f, 0.f};
#pragma unroll
  for (int ks = 0; ks < 2; ++ks) ab = __builtin_amdgcn_mfma_f32_16x16x32_bf16(haf[ks], hbf[ks], ab, 0, 0, 0);
  ab *= gb;
  const int off = T2_OFF + ((j * 32 + h) * 160 + 16 * nt + lane) * 16;
  const int aoff = L_OA + (lane & 3) * 400 + (lane >> 4) * 16;
  asm volatile("" : "+v"(ab), "+v"(bf[0]), "+v"(bf[1]), "+v"(bf[2]), "+v"(bf[3]), "+v"(bf[4]), "+v"(bf[5]));
  ctl_wait<0>(lds, C_OA, 6u);
#pragma unroll
  for (int ks = 0; ks < 6; ++ks) af[ks] = *(const bf16x8*)(lds + aoff + ks * 64);
  f32x4 aa = {0.f, 0.f, 0.f, 0.f}, aa2 = {0.f, 0.f, 0.f, 0.f};
#pragma unroll
  for (int ks = 0; ks < 3; ++ks) { aa = __builtin_amdgcn_mfma_f32_16x16x32_bf16(af[ks], bf[ks], aa, 0, 0, 0); aa2 = __builtin_amdgcn_mfma_f32_16x16x32_bf16(af[ks + 3], bf[ks + 3], aa2, 0, 0, 0); }
  if (lane < 16) {
    const f32x4 v = ga * (aa + aa2) + ab;
    STSC(mkunit(v[0], v[1], tag), rs, off); STSC(mkunit(v[2], v[3], tag), rs, off + 80 * 16);
  }
}

__device__ __forceinline__ void final_part(unsigned char* lds, __amdgpu_buffer_rsrc_t rs, int i, int lane, int h, int j, unsigned tag, unsigned short* out) {
  const int p = 2 * i + (lane >> 5);
  const int o0 = T2_OFF + ((j * 32 + (lane & 31)) * 160 + 5 * h + p) * 16;
  const int u5 = 5 * h + p, bp = u5 >= 80 ? 1 : 0, cc = u5 - bp * 80;
  unsigned short* dst = out + (2 * bp) * DM + 80 * j + cc;
  ctl_wait<1>(lds, C_OA, 6u);
  u32x4 R = {0u, 0u, 0u, 0u};
  bool done = p >= 5;
  for (unsigned spin = 0; spin < SPIN_CAP; ++spin) {
    if (!done) { R = LDSC(rs, o0); done = okunit(R, tag); }
    if (__all(done)) break;
    __builtin_amdgcn_s_sleep(T2SLEEP);
  }
  float v0 = u2f(R[0]), v1 = u2f(R[2]);
  v0 += dpp<0xB1>(v0); v1 += dpp<0xB1>(v1); v0 += dpp<0x4E>(v0); v1 += dpp<0x4E>(v1);
  v0 += dpp<0x141>(v0); v1 += dpp<0x141>(v1); v0 += dpp<0x140>(v0); v1 += dpp<0x140>(v1);
  const float t = swap16_add(v0, v1);
  const unsigned pk = cvtpk(t, t);
  if ((lane & 15) == 0 && p < 5) dst[(lane & 16) ? DM : 0] = (unsigned short)(pk & 0xffffu);
}

__global__ void __launch_bounds__(NTHR, 3)
fused_kernel(const unsigned short* __restrict__ x, const unsigned short* __restrict__ kc, const unsigned short* __restrict__ vc,
             const unsigned short* __restrict__ wq, const unsigned short* __restrict__ wo, const unsigned short* __restrict__ w1,
             const unsigned short* __restrict__ w2, const unsigned short* __restrict__ gw, unsigned short* __restrict__ out,
             unsigned char* ws
             ) {
  __shared__ __attribute__((aligned(128))) unsigned char lds[L_END];
  asm volatile("" :: "s"(x), "s"(kc), "s"(vc), "s"(wq), "s"(wo), "s"(w1), "s"(w2), "s"(gw), "s"(out), "s"(ws));
  const int wg = blockIdx.x;
  const int xcd = wg & 7, ii = wg >> 3, h = xcd * 4 + (ii >> 3), j = ii & 7, b = j >> 1, half = j & 1;
  const int tid = threadIdx.x, lane = tid & 63, wave = __builtin_amdgcn_readfirstlane(tid >> 6);
  const __amdgpu_buffer_rsrc_t rs = __builtin_amdgcn_make_buffer_rsrc((void*)ws, 0, WS_USED, 0x00020000);

  if (wave >= 4) {
    const int w = wave - 4, r = lane >> 3, c = lane & 7;
    if (w == 0) STAMP(0);
    const size_t base = ((size_t)(b * NH + h) * SK + (SK - WIN) + half * 512) * DH * 2;
    const __amdgpu_buffer_rsrc_t rk = __builtin_amdgcn_make_buffer_rsrc((void*)((const char*)kc + base), 0, 512 * 384, 0x00020000);
    const __amdgpu_buffer_rsrc_t rv = __builtin_amdgcn_make_buffer_rsrc((void*)((const char*)vc + base), 0, 512 * 384, 0x00020000);
    const int voff = w * 3072 + r * 384 + c * 16;
    u32x4 K[8][3], V[8][3];
#pragma unroll
    for (int g = 0; g < 8; ++g)
#pragma unroll
      for (int t = 0; t < 3; ++t) {
        if (g * 3 + t == DEPTH) { BARRIER(); }
        if (g * 3 + t >= DEPTH) waitvm<DEPTH - 1>();
        K[g][t] = LDKV(rk, voff + t * 128, g * 24576); if (PACE) __builtin_amdgcn_s_sleep(PACE); SB();
      }
#pragma unroll
    for (int g = 0; g < NVPRE; ++g)
#pragma unroll
      for (int t = 0; t < 3; ++t) { waitvm<DEPTH - 1>(); V[g][t] = LDKV(rv, voff + t * 128, g * 24576); if (PACE) __builtin_amdgcn_s_sleep(PACE); SB(); }
    if (w == 0) STAMP(2);
    ctl_wait<1>(lds, C_Q, 2u);
    u32x4 qf[3];
#pragma unroll
    for (int t = 0; t < 3; ++t) qf[t] = *(const u32x4*)(lds + L_QS + t * 128 + c * 16);
    float s[8];
#pragma unroll
    for (int g = 0; g < 8; ++g) {
      float d0 = 0.f, d1 = 0.f, d2 = 0.f;
#pragma unroll
      for (int k = 0; k < 4; ++k) { d0 = dot2(K[g][0][k], qf[0][k], d0); d1 = dot2(K[g][1][k], qf[1][k], d1); d2 = dot2(K[g][2][k], qf[2][k], d2); }
      float d = (d0 + d1) + d2;
      asm volatile("" : "+v"(d));
      SB();
      if (g == 0) {
#pragma unroll
        for (int t = 0; t < 3; ++t) { V[NVPRE][t] = LDKV(rv, voff + t * 128, NVPRE * 24576); SB(); }
      }
      d += dpp<0xB1>(d); d += dpp<0x4E>(d); d += dpp<0x141>(d);
      s[g] = d * (0.07216878364870322f * 1.4426950408889634f);
    }
    float m = fmaxf(fmaxf(fmaxf(s[0], s[1]), fmaxf(s[2], s[3])), fmaxf(fmaxf(s[4], s[5]), fmaxf(s[6], s[7])));
    m = fmaxf(m, dpp<0x128>(m)); m = swap16_max(m); m = swap32_max(m);
    if (lane == 0) *(volatile LAS u32x2*)(lds + L_MISC + C_MX + w * 8) = u32x2{f2u(m), 1u};
    float l = 0.f;
    f32x2 acc[12];
#pragma unroll
    for (int i = 0; i < 12; ++i) acc[i] = f32x2{0.f, 0.f};
#pragma unroll
    for (int g = 0; g < 8; ++g) {
      if (g + NVPRE + 1 < 8) {
        waitvm<PVW>();
#pragma unroll
        for (int t = 0; t < 3; ++t) { V[g + NVPRE + 1][t] = LDKV(rv, voff + t * 128, (g + NVPRE + 1) * 24576); if (PACE2) __builtin_amdgcn_s_sleep(PACE2); SB(); }
        if (g + NVPRE + 1 == 7 && lane == 0) __hip_atomic_fetch_add((LAS unsigned*)(lds + L_MISC + C_VISS), 1u, __ATOMIC_RELAXED, __HIP_MEMORY_SCOPE_WORKGROUP);
      }
      if (g == 3) {
        u32x4 ma, mb, mc, md;
        for (unsigned spin = 0; spin < LSPIN; ++spin) {
          ma = *(volatile const LAS u32x4*)(lds + L_MISC + C_MX); mb = *(volatile const LAS u32x4*)(lds + L_MISC + C_MX + 16);
          mc = *(volatile const LAS u32x4*)(lds + L_MISC + C_MX + 32); md = *(volatile const LAS u32x4*)(lds + L_MISC + C_MX + 48);
          if ((ma[1] & ma[3] & mb[1] & mb[3] & mc[1] & mc[3] & md[1] & md[3]) != 0u) break;
          __builtin_amdgcn_s_sleep(1);
        }
        const float M = fmaxf(fmaxf(fmaxf(u2f(ma[0]), u2f(ma[2])), fmaxf(u2f(mb[0]), u2f(mb[2]))), fmaxf(fmaxf(u2f(mc[0]), u2f(mc[2])), fmaxf(u2f(md[0]), u2f(md[2]))));
        const float f = __builtin_amdgcn_exp2f(m - M);
        m = M; l *= f;
        const f32x2 f2 = {f, f};
#pragma unroll
        for (int i = 0; i < 12; ++i) acc[i] *= f2;
      }
      const float p = __builtin_amdgcn_exp2f(s[g] - m);
      l += p;
      const f32x2 p2 = {p, p};
#pragma unroll
      for (int t = 0; t < 3; ++t)
#pragma unroll
        for (int k = 0; k < 4; ++k) { const f32x2 v2 = {bf_lo(V[g][t][k]), bf_hi(V[g][t][k])}; acc[t * 4 + k] = __builtin_elementwise_fma(p2, v2, acc[t * 4 + k]); }
      asm volatile("" : "+v"(acc[0]), "+v"(acc[1]), "+v"(acc[2]), "+v"(acc[3]), "+v"(acc[4]), "+v"(acc[5]), "+v"(acc[6]), "+v"(acc[7]), "+v"(acc[8]), "+v"(acc[9]), "+v"(acc[10]), "+v"(acc[11]), "+v"(l));
      SB();
    }
    {
      float t1[12], t2[6];
#pragma unroll
      for (int a = 0; a < 12; ++a) t1[a] = swap32_add(acc[a >> 1][a & 1], acc[(a + 12) >> 1][a & 1]);
#pragma unroll
      for (int a = 0; a < 6; ++a) t2[a] = swap16_add(t1[a], t1[a + 6]);
#pragma unroll
      for (int a = 0; a < 6; ++a) t2[a] += dpp<0x128>(t2[a]);
      l += dpp<0x128>(l); l = swap16_add(l, l); l = swap32_add(l, l);
      const int rho = lane >> 4;
      if ((lane & 8) == 0) {
        float* red = (float*)(lds + L_RED) + w * 192;
#pragma unroll
        for (int i2 = 0; i2 < 3; ++i2) { const int a = 6 * rho + 2 * i2; *(f32x2*)(red + (a >> 3) * 64 + c * 8 + (a & 7)) = f32x2{t2[2 * i2], t2[2 * i2 + 1]}; }
      }
      if (lane == 0) *(float*)(lds + L_MISC + C_LS + w * 4) = l;
    }
    ctl_inc(lds, C_PV, lane);
    if (w == 0) STAMP(4);
    const unsigned tag = ctl_ld(lds, C_TAG);
    if (w >= 6) {
      const int u = (w - 6) * 64 + lane;
      const int toff = T1_OFF + ((h * 8 + j) * 104 + u) * 16;
      ctl_wait<0>(lds, C_PV, 8u);
      if (u < 96) {
        const float* red = (const float*)(lds + L_RED) + 2 * u;
        f32x2 o = *(const f32x2*)red;
#pragma unroll
        for (int ww = 1; ww < 8; ++ww) o += *(const f32x2*)(red + ww * 192);
        STSC(mkunit(o[0], o[1], tag), rs, toff);
      } else if (u < 104) {
        const f32x4 la = *(const f32x4*)(lds + L_MISC + C_LS), lb = *(const f32x4*)(lds + L_MISC + C_LS + 16);
        const float L = ((la[0] + la[1]) + (la[2] + la[3])) + ((lb[0] + lb[1]) + (lb[2] + lb[3]));
        STSC(mkunit(m, L, tag), rs, toff);
      }
      if (w == 6) {
        STAMP(11);
        outproj_tile(lds, rs, 4, lane, h, j, tag);
      } else {
        final_part(lds, rs, 0, lane, h, j, tag, out);
        if (wg == 0 && lane == 0) __hip_atomic_store((GAS unsigned*)ws, tag, __ATOMIC_RELAXED, __HIP_MEMORY_SCOPE_AGENT);
        STAMP(7);
      }
    } else {
      const int it = w * 64 + lane, bb = it / 96, p = it - bb * 96;
      const int o0 = T1_OFF + ((h * 8 + 2 * bb) * 104) * 16, o1 = o0 + 104 * 16;
      const int pa0 = o0 + p * 16, pm0 = o0 + 96 * 16, pa1 = o1 + p * 16, pm1 = o1 + 96 * 16;
      u32x4 R[4];
#pragma unroll
      for (int i = 0; i < 4; ++i) R[i] = u32x4{0u, 0u, 0u, 0u};
      ctl_wait<1>(lds, C_PV, 8u);
      {
        const int bbl = (w * 64) / 96, bbh = (w * 64 + 63) / 96;
        const int pj = (lane & 1) + 2 * ((lane & 2) ? bbh : bbl);
        const int so = T1_OFF + ((h * 8 + pj) * 104 + 96) * 16;
        const int nl = bbl == bbh ? 2 : 4;
        bool ok = lane >= nl;
        for (unsigned spin = 0; spin < SPIN_CAP; ++spin) {
          if (!ok) { const u32x4 sn = LDSC(rs, so); ok = okunit(sn, tag); }
          if (__popcll(__ballot(!ok)) <= T1DIR) break;
          __builtin_amdgcn_s_sleep(T1SLEEP);
        }
        bool ok0 = false, ok1 = false;
        for (unsigned spin = 0; spin < SPIN_CAP; ++spin) {
          if (!ok0) { R[0] = LDSC(rs, pa0); R[1] = LDSC(rs, pm0); }
          if (!ok1) { R[2] = LDSC(rs, pa1); R[3] = LDSC(rs, pm1); }
          ok0 = okunit(R[0], tag) && okunit(R[1], tag); ok1 = okunit(R[2], tag) && okunit(R[3], tag);
          if (__all(ok0 && ok1)) break;
          if (T1SLEEP2) __builtin_amdgcn_s_sleep(T1SLEEP2);
        }
      }
      const float M0 = u2f(R[1][0]), M1 = u2f(R[3][0]), M = fmaxf(M0, M1);
      const float e0 = __builtin_amdgcn_exp2f(M0 - M), e1 = __builtin_amdgcn_exp2f(M1 - M);
      const float inv = __builtin_amdgcn_rcpf(e0 * u2f(R[1][2]) + e1 * u2f(R[3][2]));
      const float f0 = e0 * inv, f1 = e1 * inv;
      *(unsigned*)(lds + L_OA + bb * 400 + p * 4) = cvtpk(f0 * u2f(R[0][0]) + f1 * u2f(R[2][0]), f0 * u2f(R[0][2]) + f1 * u2f(R[2][2]));
      ctl_inc(lds, C_OA, lane);
      if (w == 0) STAMP(12);
      if (w >= 4) final_part(lds, rs, w - 3, lane, h, j, tag, out);
    }
    STAMP_FLUSH();
  } else {
    __builtin_amdgcn_s_setprio(3);
    const int hw = wave, r = lane >> 3, c = lane & 7;
    const unsigned tag = __hip_atomic_load((GAS unsigned*)ws, __ATOMIC_RELAXED, __HIP_MEMORY_SCOPE_AGENT) + 1u;
    unsigned xp[3];
#pragma unroll
    for (int i = 0; i < 3; ++i) { int id = lane + 64 * i; id = id < 160 ? id : 159; const int bb = id / 40, kp = id - bb * 40;
      xp[i] = *(const GAS unsigned*)(x + bb * DM + 80 * j + 2 * kp); }
    SB();
    u32x4 W[10];
    {
      const int ce = (hw == 3 && c >= 6) ? 5 : c;
      const unsigned short* p0 = hw < 3 ? (wq + (size_t)(80 * j + 2 * r) * QD + h * DH + hw * 64 + ce * 8) : (w1 + (size_t)(80 * j + 2 * r) * DFF + h * 48 + ce * 8);
      const int rs1 = hw < 3 ? QD : DFF;
#pragma unroll
      for (int m = 0; m < 5; ++m)
#pragma unroll
        for (int sub = 0; sub < 2; ++sub) { W[2 * m + sub] = ldnt(p0 + (size_t)((16 * m + sub) * rs1)); SB(); }
    }
    if (hw == 0) ctl_st(lds, lane * 4, 0u);
    BARRIER();
#pragma unroll
    for (int i = 0; i < 3; ++i) { const int id = lane + 64 * i; if (id < 160) { const int bb = id / 40, kp = id - bb * 40; *(unsigned*)(lds + L_XS + hw * 640 + kp * 16 + bb * 4) = xp[i]; } }
    if (hw == 0 && lane == 0) ctl_st(lds, C_TAG, tag);
    {
      float acc[32];
#pragma unroll
      for (int i = 0; i < 32; ++i) acc[i] = 0.f;
#pragma unroll
      for (int m = 0; m < 5; ++m) {
        const u32x4 xq = *(const u32x4*)(lds + L_XS + hw * 640 + (8 * m + r) * 16);
#pragma unroll
        for (int d = 0; d < 4; ++d) {
          const unsigned lo = __builtin_amdgcn_perm(W[2 * m + 1][d], W[2 * m][d], 0x05040100u);
          const unsigned hi = __builtin_amdgcn_perm(W[2 * m + 1][d], W[2 * m][d], 0x07060302u);
#pragma unroll
          for (int bb = 0; bb < 4; ++bb) { acc[bb * 8 + 2 * d] = dot2(lo, xq[bb], acc[bb * 8 + 2 * d]); acc[bb * 8 + 2 * d + 1] = dot2(hi, xq[bb], acc[bb * 8 + 2 * d + 1]); }
        }
      }
      float t[16], u[8];
#pragma unroll
      for (int i = 0; i < 16; ++i) t[i] = swap32_add(acc[i], acc[i + 16]);
#pragma unroll
      for (int i = 0; i < 8; ++i) u[i] = swap16_add(t[i], t[i + 8]);
#pragma unroll
      for (int i = 0; i < 8; ++i) u[i] += dpp<0x128>(u[i]);
      const int bb = lane >> 4, hi4 = (lane >> 3) & 1;
      const float v0 = hi4 ? u[4] : u[0], v1 = hi4 ? u[5] : u[1], v2 = hi4 ? u[6] : u[2], v3 = hi4 ? u[7] : u[3];
      const int off = hw < 3 ? (T0Q_OFF + (((h * 4 + bb) * 8 + j) * 96 + hw * 32 + (lane & 15)) * 16)
                             : (T0H_OFF + (((h * 8 + j) * 4 + bb) * 32 + (lane & 15)) * 16);
      __builtin_amdgcn_raw_buffer_store_b128(mkunit(v0, v1, tag), rs, off, 0, T0AUX); __builtin_amdgcn_raw_buffer_store_b128(mkunit(v2, v3, tag), rs, off + 256, 0, T0AUX);
    }
    if (hw == 0) STAMP(9);
    if (hw == 0) {
      __builtin_amdgcn_s_sleep(T0DELAY);
      const int so = lane < 8 ? (T0Q_OFF + (((h * 4 + b) * 8 + lane) * 96 + 95) * 16) : (T0H_OFF + (((h * 8 + (lane & 7)) * 4 + 3) * 32 + 16) * 16);
      bool ok = lane >= 16;
      for (unsigned spin = 0; spin < SPIN_CAP; ++spin) {
        if (!ok) { const u32x4 sn = LDSC(rs, so); ok = okunit(sn, tag); }
        if (__all(ok)) break;
        __builtin_amdgcn_s_sleep(T0SLEEP);
      }
      if (lane == 0) ctl_st(lds, C_T0, 1u);
    } else if (hw == 2) {
      u32x4 G[10];
      const int l16 = lane < 16 ? lane + 64 : lane;
      G[0] = *(const GAS u32x4*)(gw + lane * 8); G[1] = *(const GAS u32x4*)(gw + l16 * 8);
#pragma unroll
      for (int bb = 0; bb < 4; ++bb) { G[2 + 2 * bb] = *(const GAS u32x4*)(x + bb * DM + lane * 8); G[3 + 2 * bb] = *(const GAS u32x4*)(x + bb * DM + l16 * 8); }
#pragma unroll
      for (int bb = 0; bb < 4; ++bb) {
        float s0 = 0.f, s1 = 0.f;
#pragma unroll
        for (int d = 0; d < 4; ++d) {
          s0 += rbf(bf_lo(G[2 + 2 * bb][d]) * bf_lo(G[0][d])); s0 += rbf(bf_hi(G[2 + 2 * bb][d]) * bf_hi(G[0][d]));
          s1 += rbf(bf_lo(G[3 + 2 * bb][d]) * bf_lo(G[1][d])); s1 += rbf(bf_hi(G[3 + 2 * bb][d]) * bf_hi(G[1][d]));
        }
        float t = s0 + (lane < 16 ? s1 : 0.f);
        t += dpp<0xB1>(t); t += dpp<0x4E>(t); t += dpp<0x141>(t); t += dpp<0x140>(t);
        t = swap16_add(t, t); t = swap32_add(t, t);
        t = rbf(t);
        const float g = rbf(1.f / (1.f + __expf(-t)));
        if (lane == 0) { *(float*)(lds + L_MISC + C_G + bb * 4) = g; *(float*)(lds + L_MISC + C_OMG + bb * 4) = rbf(1.f - g); }
      }
    } else if (hw == 3) {
      u32x4 W2[8];
      const int l60 = lane < 60 ? lane : 59, rb = l60 / 10, cq = l60 - rb * 10;
      const unsigned short* p2 = w2 + (size_t)(48 * h + 8 * rb) * DM + 80 * j + cq * 8;
#pragma unroll
      for (int i = 0; i < 8; ++i) W2[i] = ldnt(p2 + i * DM);
      if (lane < 60) {
        const int ks = rb >> 2, kg = rb & 3, nt = cq >> 1;
        const int dst = L_W2B + ((ks * 5 + nt) * 64 + kg * 16 + (cq & 1) * 8 + (cq & 7)) * 16;
#pragma unroll
        for (int cc = 0; cc < 8; ++cc) {
          const unsigned sel = (cc & 1) ? 0x07060302u : 0x05040100u;
          u32x4 o;
#pragma unroll
          for (int kk = 0; kk < 4; ++kk) o[kk] = __builtin_amdgcn_perm(W2[2 * kk + 1][cc >> 1], W2[2 * kk][cc >> 1], sel);
          *(u32x4*)(lds + (dst ^ (cc * 16))) = o;
        }
      }
#pragma unroll
      for (int i = 0; i < 3; ++i) { const int id = lane + 64 * i; if (id < 160) *(u32x4*)(lds + L_W2B + ((5 + id / 32) * 64 + 32 + (id & 31)) * 16) = u32x4{0u, 0u, 0u, 0u}; }
      if (lane < 32) *(unsigned*)(lds + L_HA + (lane >> 3) * 144 + (24 + (lane & 7)) * 4) = 0u;
    }
    ctl_wait<2>(lds, C_T0, 1u);
    {
      const int gi = (hw & 1) * 64 + lane; const bool live = gi < 96; const int gl = live ? gi : 95;
      const int hb = gl / 24, hq = gl - hb * 24, hsel = hq / 12, q2 = hq - hsel * 12, h4 = q2 / 6, hc = q2 - h4 * 6;
      const int off0 = hw < 2 ? (T0Q_OFF + ((h * 4 + b) * 8 * 96 + gl) * 16) : (T0H_OFF + ((h * 8 * 4 + hb) * 32 + hsel * 16 + h4 * 8 + hc) * 16);
      const int jstr = hw < 2 ? 1536 : 2048;
      u32x4 U[8];
      for (unsigned spin = 0; spin < SPIN_CAP; ++spin) {
        bool ok = true;
#pragma unroll
        for (int jj = 0; jj < 8; ++jj) U[jj] = LDSC(rs, off0 + jj * jstr);
#pragma unroll
        for (int jj = 0; jj < 8; ++jj) ok = ok && okunit(U[jj], tag);
        if (__all(ok)) break;
        __builtin_amdgcn_s_sleep(T0SLEEP);
      }
      float v0 = 0.f, v1 = 0.f;
#pragma unroll
      for (int jj = 0; jj < 8; ++jj) { v0 += u2f(U[jj][0]); v1 += u2f(U[jj][2]); }
      if (hw < 2) {
        const int blk = gl >> 5, pos = gl & 31, sel = pos >> 4, p4 = (pos >> 3) & 1, cc = pos & 7;
        if (live) *(unsigned*)(lds + L_QS + (blk * 32 + cc * 4 + p4 * 2 + sel) * 4) = cvtpk(v0, v1);
        ctl_inc(lds, C_Q, lane);
        if (hw == 0) STAMP(1);
      } else {
        if (live) *(unsigned*)(lds + L_HA + hb * 144 + (hc * 4 + h4 * 2 + hsel) * 4) = cvtpk(fmaxf(v0, 0.f), fmaxf(v1, 0.f));
        ctl_inc(lds, C_FFN, lane);
      }
    }
    ctl_wait<WOSL>(lds, WOSRC, WO_WAIT);
    if (hw == 0) STAMP(14);
    {
      u32x4 WO[8];
      const int l60 = lane < 60 ? lane : 59, rbl = l60 / 10, cq = l60 - rbl * 10;
      const unsigned short* po = wo + (size_t)(192 * h + 8 * (6 * hw + rbl)) * DM + 80 * j + cq * 8;
#pragma unroll
      for (int i = 0; i < 8; ++i) { WO[i] = ldnt(po + i * DM); SB(); }
      if (lane < 60) {
        const int rb = 6 * hw + rbl, ks = rb >> 2, kg = rb & 3, nt = cq >> 1;
        const int dst = L_WB + ((ks * 5 + nt) * 64 + kg * 16 + (cq & 1) * 8 + (cq & 7)) * 16;
#pragma unroll
        for (int cc = 0; cc < 8; ++cc) {
          const unsigned sel = (cc & 1) ? 0x07060302u : 0x05040100u;
          u32x4 o;
#pragma unroll
          for (int kk = 0; kk < 4; ++kk) o[kk] = __builtin_amdgcn_perm(WO[2 * kk + 1][cc >> 1], WO[2 * kk][cc >> 1], sel);
          *(u32x4*)(lds + (dst ^ (cc * 16))) = o;
        }
      }
    }
    ctl_inc(lds, C_TILE, lane);
    if (hw == 0) STAMP(13);
    outproj_tile(lds, rs, hw, lane, h, j, tag);
    if (hw == 0) STAMP(6);
    STAMP_FLUSH();
  }
}

extern "C" void kernel_launch(void* const* d_in, const int* in_sizes, int n_in, void* d_out, int out_size, void* d_ws, size_t ws_size, hipStream_t stream) {
  fused_kernel<<<256, NTHR, 0, stream>>>((const unsigned short*)d_in[0], (const unsigned short*)d_in[1], (const unsigned short*)d_in[2],
      (const unsigned short*)d_in[3], (const unsigned short*)d_in[4], (const unsigned short*)d_in[6], (const unsigned short*)d_in[7],
      (const unsigned short*)d_in[8], (unsigned short*)d_out, (unsigned char*)d_ws
      );
}
```

```cpp
#include <hip/hip_runtime.h>
#include <hip/hip_bf16.h>
#include <stdint.h>

typedef unsigned u32x4 __attribute__((ext_vector_type(4)));
typedef unsigned u32x2 __attribute__((ext_vector_type(2)));
typedef float f32x4 __attribute__((ext_vector_type(4)));
typedef float f32x2 __attribute__((ext_vector_type(2)));
typedef short bf16x8 __attribute__((ext_vector_type(8)));
typedef __bf16 bf16x2_t __attribute__((ext_vector_type(2)));
#define GAS __attribute__((address_space(1)))
#define LAS __attribute__((address_space(3)))

constexpr int DEPTH = 6;
constexpr int PACE = 11;
constexpr int NVPRE = 4;
constexpr int PVW = 4;
constexpr int T0DELAY = 24, T0SLEEP = 12, T1SLEEP = 4, T2SLEEP = 4;
constexpr int DM = 640, NH = 32, SK = 4096, DH = 192, WIN = 1024, DFF = 1536, QD = NH * DH;
constexpr int NTHR = 768;
constexpr int T0F_OFF = 4096;
constexpr int T0Q_OFF = T0F_OFF + 32 * 8 * 128;
constexpr int T0H_OFF = T0Q_OFF + 32 * 24576;
constexpr int T1_OFF = T0H_OFF + 32 * 8 * 768;
constexpr int WS_USED = T1_OFF + 32 * 8 * 104 * 16;
constexpr unsigned SPIN_CAP = 1u << 15;
constexpr unsigned LSPIN = 1u << 22;
constexpr int L_XS = 0;
constexpr int L_QS = 2560;
constexpr int L_MISC = 2944;
constexpr int L_HA = 3200;
constexpr int L_OA = 3776;
constexpr int L_RED = 5376;
constexpr int L_WB = 11520;
constexpr int L_W2B = 42240;
constexpr int L_END = 52480;
constexpr int C_TAG = 0, C_VISS = 4, C_Q = 8, C_PV = 12, C_TILE = 16, C_FFN = 20, C_OA = 24, C_T0 = 28, C_DR = 32, C_LS = 96, C_G = 128, C_OMG = 144, C_MX = 160;


__device__ __forceinline__ float bf_lo(unsigned u) { return __builtin_bit_cast(float, u << 16); }
__device__ __forceinline__ float bf_hi(unsigned u) { return __builtin_bit_cast(float, u & 0xffff0000u); }
__device__ __forceinline__ unsigned cvtpk(float lo, float hi) {
  unsigned r; asm("v_cvt_pk_bf16_f32 %0, %1, %2" : "=v"(r) : "v"(lo), "v"(hi)); return r;
}
__device__ __forceinline__ float rbf(float f) { return bf_lo(cvtpk(f, f)); }
__device__ __forceinline__ float dot2(unsigned a, unsigned b, float acc) {
  return __builtin_amdgcn_fdot2_f32_bf16(__builtin_bit_cast(bf16x2_t, a), __builtin_bit_cast(bf16x2_t, b), acc, false);
}
template <int CTRL> __device__ __forceinline__ float dpp(float x) {
  return __builtin_bit_cast(float, __builtin_amdgcn_mov_dpp(__builtin_bit_cast(int, x), CTRL, 0xf, 0xf, true));
}
__device__ __forceinline__ float u2f(unsigned u) { return __builtin_bit_cast(float, u); }
__device__ __forceinline__ unsigned f2u(float f) { return __builtin_bit_cast(unsigned, f); }
__device__ __forceinline__ float swap32_add(float a, float b) {
  auto s = __builtin_amdgcn_permlane32_swap(f2u(a), f2u(b), false, false); return u2f(s[0]) + u2f(s[1]);
}
__device__ __forceinline__ float swap16_add(float a, float b) {
  auto s = __builtin_amdgcn_permlane16_swap(f2u(a), f2u(b), false, false); return u2f(s[0]) + u2f(s[1]);
}
__device__ __forceinline__ float swap32_max(float a) {
  auto s = __builtin_amdgcn_permlane32_swap(f2u(a), f2u(a), false, false); return fmaxf(u2f(s[0]), u2f(s[1]));
}
__device__ __forceinline__ float swap16_max(float a) {
  auto s = __builtin_amdgcn_permlane16_swap(f2u(a), f2u(a), false, false); return fmaxf(u2f(s[0]), u2f(s[1]));
}
#define SB() __builtin_amdgcn_sched_barrier(0)
#define BARRIER() asm volatile("s_waitcnt lgkmcnt(0)\n\ts_barrier" ::: "memory")
#define LGKM0() asm volatile("s_waitcnt lgkmcnt(0)" ::: "memory")
#define CBAR() asm volatile("" ::: "memory")
template <int N> __device__ __forceinline__ void waitvm() { asm volatile("s_waitcnt vmcnt(%0)" ::"n"(N)); }
#define LDKV(rs, vo, so) __builtin_amdgcn_raw_buffer_load_b128(rs, vo, so, 2)
__device__ __forceinline__ int opq(int v) { asm volatile("" : "+v"(v)); return v; }
#define LDSC(rs, vo) __builtin_amdgcn_raw_buffer_load_b128(rs, opq(vo), 0, 16)
#define STSC(v, rs, vo) __builtin_amdgcn_raw_buffer_store_b128(v, rs, vo, 0, 16)
__device__ __forceinline__ u32x4 ldnt(const unsigned short* p) { return __builtin_nontemporal_load((const GAS u32x4*)p); }
__device__ __forceinline__ u32x4 mkunit(float a, float b, unsigned tag) { return u32x4{f2u(a), tag, f2u(b), tag}; }
__device__ __forceinline__ bool okunit(u32x4 u, unsigned tag) { return u[1] == tag && u[3] == tag; }
__device__ __forceinline__ unsigned ctl_ld(const unsigned char* lds, int off) { return *(volatile const LAS unsigned*)(lds + L_MISC + off); }
__device__ __forceinline__ void ctl_st(unsigned char* lds, int off, unsigned v) { *(volatile LAS unsigned*)(lds + L_MISC + off) = v; }
__device__ __forceinline__ void ctl_inc(unsigned char* lds, int off, int lane) {
  LGKM0();
  if (lane == 0) __hip_atomic_fetch_add((LAS unsigned*)(lds + L_MISC + off), 1u, __ATOMIC_RELAXED, __HIP_MEMORY_SCOPE_WORKGROUP);
}
template <int SLEEP> __device__ __forceinline__ void ctl_wait(const unsigned char* lds, int off, unsigned want) {
  for (unsigned spin = 0; spin < LSPIN; ++spin) { if (ctl_ld(lds, off) >= want) break; if (SLEEP) __builtin_amdgcn_s_sleep(SLEEP); }
  CBAR();
}

__device__ __forceinline__ void outproj_tile(unsigned char* lds, __amdgpu_buffer_rsrc_t rs, int nt, int lane, int h, int j, unsigned tag) {
  ctl_wait<1>(lds, C_TILE, 4u);
  ctl_wait<1>(lds, C_FFN, 2u);
  const int slot = (lane & 56) | ((lane & 7) ^ ((2 * nt + ((lane >> 3) & 1)) & 7));
  bf16x8 bf[6], haf[2], hbf[2], af[6];
#pragma unroll
  for (int ks = 0; ks < 6; ++ks) bf[ks] = *(const bf16x8*)(lds + L_WB + ((ks * 5 + nt) * 64 + slot) * 16);
#pragma unroll
  for (int ks = 0; ks < 2; ++ks) {
    haf[ks] = *(const bf16x8*)(lds + L_HA + (lane & 3) * 144 + ks * 64 + (lane >> 4) * 16);
    hbf[ks] = *(const bf16x8*)(lds + L_W2B + ((ks * 5 + nt) * 64 + slot) * 16);
  }
  const f32x4 ga = *(const f32x4*)(lds + L_MISC + C_G), gb = *(const f32x4*)(lds + L_MISC + C_OMG);
  f32x4 ab = {0.f, 0.f, 0.f, 0.f};
#pragma unroll
  for (int ks = 0; ks < 2; ++ks) ab = __builtin_amdgcn_mfma_f32_16x16x32_bf16(haf[ks], hbf[ks], ab, 0, 0, 0);
  ab *= gb;
  const int off = T0Q_OFF + h * 24576 + j * 2560 + (16 * nt + lane) * 16;
  const int aoff = L_OA + (lane & 3) * 400 + (lane >> 4) * 16;
  asm volatile("" : "+v"(ab), "+v"(bf[0]), "+v"(bf[1]), "+v"(bf[2]), "+v"(bf[3]), "+v"(bf[4]), "+v"(bf[5]));
  ctl_wait<0>(lds, C_OA, 6u);
#pragma unroll
  for (int ks = 0; ks < 6; ++ks) af[ks] = *(const bf16x8*)(lds + aoff + ks * 64);
  f32x4 aa = {0.f, 0.f, 0.f, 0.f}, aa2 = {0.f, 0.f, 0.f, 0.f};
#pragma unroll
  for (int ks = 0; ks < 3; ++ks) { aa = __builtin_amdgcn_mfma_f32_16x16x32_bf16(af[ks], bf[ks], aa, 0, 0, 0); aa2 = __builtin_amdgcn_mfma_f32_16x16x32_bf16(af[ks + 3], bf[ks + 3], aa2, 0, 0, 0); }
  if (lane < 16) {
    const f32x4 v = ga * (aa + aa2) + ab;
    STSC(mkunit(v[0], v[1], tag), rs, off); STSC(mkunit(v[2], v[3], tag), rs, off + 80 * 16);
  }
}

__device__ __forceinline__ void final_part(unsigned char* lds, __amdgpu_buffer_rsrc_t rs, int i, int lane, int h, int j, unsigned tag, unsigned short* out) {
  const int p = 2 * i + (lane >> 5);
  const int o0 = T0Q_OFF + (lane & 31) * 24576 + j * 2560 + (5 * h + p) * 16;
  const int u5 = 5 * h + p, bp = u5 >= 80 ? 1 : 0, cc = u5 - bp * 80;
  unsigned short* dst = out + (2 * bp) * DM + 80 * j + cc;
  ctl_wait<1>(lds, C_OA, 6u);
  u32x4 R = {0u, 0u, 0u, 0u};
  bool done = p >= 5;
  for (unsigned spin = 0; spin < SPIN_CAP; ++spin) {
    if (!done) { R = LDSC(rs, o0); done = okunit(R, tag); }
    if (__all(done)) break;
    __builtin_amdgcn_s_sleep(T2SLEEP);
  }
  float v0 = u2f(R[0]), v1 = u2f(R[2]);
  v0 += dpp<0xB1>(v0); v1 += dpp<0xB1>(v1); v0 += dpp<0x4E>(v0); v1 += dpp<0x4E>(v1);
  v0 += dpp<0x141>(v0); v1 += dpp<0x141>(v1); v0 += dpp<0x140>(v0); v1 += dpp<0x140>(v1);
  const float t = swap16_add(v0, v1);
  const unsigned pk = cvtpk(t, t);
  if ((lane & 15) == 0 && p < 5) dst[(lane & 16) ? DM : 0] = (unsigned short)(pk & 0xffffu);
}

__global__ void __launch_bounds__(NTHR, 3)
fused_kernel(const unsigned short* __restrict__ x, const unsigned short* __restrict__ kc, const unsigned short* __restrict__ vc,
             const unsigned short* __restrict__ wq, const unsigned short* __restrict__ wo, const unsigned short* __restrict__ w1,
             const unsigned short* __restrict__ w2, const unsigned short* __restrict__ gw, unsigned short* __restrict__ out,
             unsigned char* ws) {
  __shared__ __attribute__((aligned(128))) unsigned char lds[L_END];
  asm volatile("" :: "s"(x), "s"(kc), "s"(vc), "s"(wq), "s"(wo), "s"(w1), "s"(w2), "s"(gw), "s"(out), "s"(ws));
  const int wg = blockIdx.x;
  const int xcd = wg & 7, ii = wg >> 3, h = xcd * 4 + (ii >> 3), j = ii & 7, b = j >> 1, half = j & 1;
  const int tid = threadIdx.x, lane = tid & 63, wave = __builtin_amdgcn_readfirstlane(tid >> 6);
  const __amdgpu_buffer_rsrc_t rs = __builtin_amdgcn_make_buffer_rsrc((void*)ws, 0, WS_USED, 0x00020000);

  if (wave >= 4) {
    const int w = wave - 4, r = lane >> 3, c = lane & 7;
    const size_t base = ((size_t)(b * NH + h) * SK + (SK - WIN) + half * 512) * DH * 2;
    const __amdgpu_buffer_rsrc_t rk = __builtin_amdgcn_make_buffer_rsrc((void*)((const char*)kc + base), 0, 512 * 384, 0x00020000);
    const __amdgpu_buffer_rsrc_t rv = __builtin_amdgcn_make_buffer_rsrc((void*)((const char*)vc + base), 0, 512 * 384, 0x00020000);
    const int voff = w * 3072 + r * 384 + c * 16;
#define GOFF(g) ((g) * 24576)
    u32x4 K[8][3], V[8][3];
#pragma unroll
    for (int g = 0; g < 8; ++g)
#pragma unroll
      for (int t = 0; t < 3; ++t) {
        if (g * 3 + t == DEPTH) { BARRIER(); }
        if (g * 3 + t >= DEPTH) waitvm<DEPTH - 1>();
        K[g][t] = LDKV(rk, voff + t * 128, GOFF(g)); __builtin_amdgcn_s_sleep(PACE); SB();
      }
#pragma unroll
    for (int g = 0; g < NVPRE; ++g)
#pragma unroll
      for (int t = 0; t < 3; ++t) { waitvm<DEPTH - 1>(); V[g][t] = LDKV(rv, voff + t * 128, GOFF(g)); __builtin_amdgcn_s_sleep(PACE); SB(); }
    ctl_wait<1>(lds, C_Q, 1u);
    u32x4 qf[3];
#pragma unroll
    for (int t = 0; t < 3; ++t) qf[t] = *(const u32x4*)(lds + L_QS + t * 128 + c * 16);
    float s[8];
#pragma unroll
    for (int g = 0; g < 8; ++g) {
      float d0 = 0.f, d1 = 0.f, d2 = 0.f;
#pragma unroll
      for (int k = 0; k < 4; ++k) { d0 = dot2(K[g][0][k], qf[0][k], d0); d1 = dot2(K[g][1][k], qf[1][k], d1); d2 = dot2(K[g][2][k], qf[2][k], d2); }
      float d = (d0 + d1) + d2;
      asm volatile("" : "+v"(d));
      SB();
      if (g == 0) {
#pragma unroll
        for (int t = 0; t < 3; ++t) { V[NVPRE][t] = LDKV(rv, voff + t * 128, GOFF(NVPRE)); SB(); }
      }
      d += dpp<0xB1>(d); d += dpp<0x4E>(d); d += dpp<0x141>(d);
      s[g] = d * (0.07216878364870322f * 1.4426950408889634f);
    }
    float m = fmaxf(fmaxf(fmaxf(s[0], s[1]), fmaxf(s[2], s[3])), fmaxf(fmaxf(s[4], s[5]), fmaxf(s[6], s[7])));
    m = fmaxf(m, dpp<0x128>(m)); m = swap16_max(m); m = swap32_max(m);
    if (lane == 0) *(volatile LAS u32x2*)(lds + L_MISC + C_MX + w * 8) = u32x2{f2u(m), 1u};
    float l = 0.f;
    f32x2 acc[12];
#pragma unroll
    for (int i = 0; i < 12; ++i) acc[i] = f32x2{0.f, 0.f};
#pragma unroll
    for (int g = 0; g < 8; ++g) {
      if (g + NVPRE + 1 < 8) {
        waitvm<PVW>();
#pragma unroll
        for (int t = 0; t < 3; ++t) { V[g + NVPRE + 1][t] = LDKV(rv, voff + t * 128, GOFF(g + NVPRE + 1)); SB(); }
        if (g + NVPRE + 1 == 7 && lane == 0) __hip_atomic_fetch_add((LAS unsigned*)(lds + L_MISC + C_VISS), 1u, __ATOMIC_RELAXED, __HIP_MEMORY_SCOPE_WORKGROUP);
      }
      if (g == 3) {
        u32x4 ma, mb, mc, md;
        for (unsigned spin = 0; spin < LSPIN; ++spin) {
          ma = *(volatile const LAS u32x4*)(lds + L_MISC + C_MX); mb = *(volatile const LAS u32x4*)(lds + L_MISC + C_MX + 16);
          mc = *(volatile const LAS u32x4*)(lds + L_MISC + C_MX + 32); md = *(volatile const LAS u32x4*)(lds + L_MISC + C_MX + 48);
          if ((ma[1] & ma[3] & mb[1] & mb[3] & mc[1] & mc[3] & md[1] & md[3]) != 0u) break;
          __builtin_amdgcn_s_sleep(1);
        }
        const float M = fmaxf(fmaxf(fmaxf(u2f(ma[0]), u2f(ma[2])), fmaxf(u2f(mb[0]), u2f(mb[2]))), fmaxf(fmaxf(u2f(mc[0]), u2f(mc[2])), fmaxf(u2f(md[0]), u2f(md[2]))));
        const float f = __builtin_amdgcn_exp2f(m - M);
        m = M; l *= f;
        const f32x2 f2 = {f, f};
#pragma unroll
        for (int i = 0; i < 12; ++i) acc[i] *= f2;
      }
      const float p = __builtin_amdgcn_exp2f(s[g] - m);
      l += p;
      const f32x2 p2 = {p, p};
#pragma unroll
      for (int t = 0; t < 3; ++t)
#pragma unroll
        for (int k = 0; k < 4; ++k) { const f32x2 v2 = {bf_lo(V[g][t][k]), bf_hi(V[g][t][k])}; acc[t * 4 + k] = __builtin_elementwise_fma(p2, v2, acc[t * 4 + k]); }
      asm volatile("" : "+v"(acc[0]), "+v"(acc[1]), "+v"(acc[2]), "+v"(acc[3]), "+v"(acc[4]), "+v"(acc[5]), "+v"(acc[6]), "+v"(acc[7]), "+v"(acc[8]), "+v"(acc[9]), "+v"(acc[10]), "+v"(acc[11]), "+v"(l));
      SB();
    }
    {
      float t1[12], t2[6];
#pragma unroll
      for (int a = 0; a < 12; ++a) t1[a] = swap32_add(acc[a >> 1][a & 1], acc[(a + 12) >> 1][a & 1]);
#pragma unroll
      for (int a = 0; a < 6; ++a) t2[a] = swap16_add(t1[a], t1[a + 6]);
#pragma unroll
      for (int a = 0; a < 6; ++a) t2[a] += dpp<0x128>(t2[a]);
      l += dpp<0x128>(l); l = swap16_add(l, l); l = swap32_add(l, l);
      const int rho = lane >> 4;
      if ((lane & 8) == 0) {
        float* red = (float*)(lds + L_RED) + w * 192;
#pragma unroll
        for (int i2 = 0; i2 < 3; ++i2) { const int a = 6 * rho + 2 * i2; *(f32x2*)(red + (a >> 3) * 64 + c * 8 + (a & 7)) = f32x2{t2[2 * i2], t2[2 * i2 + 1]}; }
      }
      if (lane == 0) *(float*)(lds + L_MISC + C_LS + w * 4) = l;
    }
    ctl_inc(lds, C_PV, lane);
    const unsigned tag = ctl_ld(lds, C_TAG);
    if (w >= 6) {
      const int u = (w - 6) * 64 + lane;
      const int toff = T1_OFF + ((h * 8 + j) * 104 + u) * 16;
      ctl_wait<0>(lds, C_PV, 8u);
      if (u < 96) {
        const float* red = (const float*)(lds + L_RED) + 2 * u;
        f32x2 o = *(const f32x2*)red;
#pragma unroll
        for (int ww = 1; ww < 8; ++ww) o += *(const f32x2*)(red + ww * 192);
        STSC(mkunit(o[0], o[1], tag), rs, toff);
      } else if (u < 104) {
        const f32x4 la = *(const f32x4*)(lds + L_MISC + C_LS), lb = *(const f32x4*)(lds + L_MISC + C_LS + 16);
        const float L = ((la[0] + la[1]) + (la[2] + la[3])) + ((lb[0] + lb[1]) + (lb[2] + lb[3]));
        STSC(mkunit(m, L, tag), rs, toff);
      }
      if (w == 6) {
        outproj_tile(lds, rs, 4, lane, h, j, tag);
      } else {
        final_part(lds, rs, 0, lane, h, j, tag, out);
        if (wg == 0 && lane == 0) __hip_atomic_store((GAS unsigned*)ws, tag, __ATOMIC_RELAXED, __HIP_MEMORY_SCOPE_AGENT);
      }
    } else {
      const int it = w * 64 + lane, bb = it / 96, p = it - bb * 96;
      const int o0 = T1_OFF + ((h * 8 + 2 * bb) * 104) * 16, o1 = o0 + 104 * 16;
      const int pa0 = o0 + p * 16, pm0 = o0 + 96 * 16, pa1 = o1 + p * 16, pm1 = o1 + 96 * 16;
      u32x4 R[4];
#pragma unroll
      for (int i = 0; i < 4; ++i) R[i] = u32x4{0u, 0u, 0u, 0u};
      ctl_wait<1>(lds, C_PV, 8u);
      {
        const int bbl = (w * 64) / 96, bbh = (w * 64 + 63) / 96;
        const int pj = (lane & 1) + 2 * ((lane & 2) ? bbh : bbl);
        const int so = T1_OFF + ((h * 8 + pj) * 104 + 96) * 16;
        const int nl = bbl == bbh ? 2 : 4;
        bool ok = lane >= nl;
        for (unsigned spin = 0; spin < SPIN_CAP; ++spin) {
          if (!ok) { const u32x4 sn = LDSC(rs, so); ok = okunit(sn, tag); }
          if (__popcll(__ballot(!ok)) <= 1) break;
          __builtin_amdgcn_s_sleep(T1SLEEP);
        }
        bool ok0 = false, ok1 = false;
        for (unsigned spin = 0; spin < SPIN_CAP; ++spin) {
          if (!ok0) { R[0] = LDSC(rs, pa0); R[1] = LDSC(rs, pm0); }
          if (!ok1) { R[2] = LDSC(rs, pa1); R[3] = LDSC(rs, pm1); }
          ok0 = okunit(R[0], tag) && okunit(R[1], tag); ok1 = okunit(R[2], tag) && okunit(R[3], tag);
          if (__all(ok0 && ok1)) break;
          __builtin_amdgcn_s_sleep(1);
        }
      }
      const float M0 = u2f(R[1][0]), M1 = u2f(R[3][0]), M = fmaxf(M0, M1);
      const float e0 = __builtin_amdgcn_exp2f(M0 - M), e1 = __builtin_amdgcn_exp2f(M1 - M);
      const float inv = __builtin_amdgcn_rcpf(e0 * u2f(R[1][2]) + e1 * u2f(R[3][2]));
      const float f0 = e0 * inv, f1 = e1 * inv;
      *(unsigned*)(lds + L_OA + bb * 400 + p * 4) = cvtpk(f0 * u2f(R[0][0]) + f1 * u2f(R[2][0]), f0 * u2f(R[0][2]) + f1 * u2f(R[2][2]));
      ctl_inc(lds, C_OA, lane);
      if (w >= 4) final_part(lds, rs, w - 3, lane, h, j, tag, out);
    }
  } else {
    __builtin_amdgcn_s_setprio(3);
    const int hw = wave, r = lane >> 3, c = lane & 7;
    const unsigned tag = __hip_atomic_load((GAS unsigned*)ws, __ATOMIC_RELAXED, __HIP_MEMORY_SCOPE_AGENT) + 1u;
    unsigned xp[3];
#pragma unroll
    for (int i = 0; i < 3; ++i) { int id = lane + 64 * i; id = id < 160 ? id : 159; const int bb = id / 40, kp = id - bb * 40;
      xp[i] = *(const GAS unsigned*)(x + bb * DM + 80 * j + 2 * kp); }
    SB();
    u32x4 W[10];
    {
      const int ce = (hw == 3 && c >= 6) ? 5 : c;
      const unsigned short* p0 = hw < 3 ? (wq + (size_t)(80 * j + 2 * r) * QD + h * DH + hw * 64 + ce * 8) : (w1 + (size_t)(80 * j + 2 * r) * DFF + h * 48 + ce * 8);
      const int rs1 = hw < 3 ? QD : DFF;
#pragma unroll
      for (int m = 0; m < 5; ++m)
#pragma unroll
        for (int sub = 0; sub < 2; ++sub) { W[2 * m + sub] = ldnt(p0 + (size_t)((16 * m + sub) * rs1)); SB(); }
    }
    if (hw == 0) ctl_st(lds, lane * 4, 0u);
    BARRIER();
#pragma unroll
    for (int i = 0; i < 3; ++i) { const int id = lane + 64 * i; if (id < 160) { const int bb = id / 40, kp = id - bb * 40; *(unsigned*)(lds + L_XS + hw * 640 + kp * 16 + bb * 4) = xp[i]; } }
    if (hw == 0 && lane == 0) ctl_st(lds, C_TAG, tag);
    {
      float acc[32];
#pragma unroll
      for (int i = 0; i < 32; ++i) acc[i] = 0.f;
#pragma unroll
      for (int m = 0; m < 5; ++m) {
        const u32x4 xq = *(const u32x4*)(lds + L_XS + hw * 640 + (8 * m + r) * 16);
#pragma unroll
        for (int d = 0; d < 4; ++d) {
          const unsigned lo = __builtin_amdgcn_perm(W[2 * m + 1][d], W[2 * m][d], 0x05040100u);
          const unsigned hi = __builtin_amdgcn_perm(W[2 * m + 1][d], W[2 * m][d], 0x07060302u);
#pragma unroll
          for (int bb = 0; bb < 4; ++bb) { acc[bb * 8 + 2 * d] = dot2(lo, xq[bb], acc[bb * 8 + 2 * d]); acc[bb * 8 + 2 * d + 1] = dot2(hi, xq[bb], acc[bb * 8 + 2 * d + 1]); }
        }
      }
      float t[16], u[8];
#pragma unroll
      for (int i = 0; i < 16; ++i) t[i] = swap32_add(acc[i], acc[i + 16]);
#pragma unroll
      for (int i = 0; i < 8; ++i) u[i] = swap16_add(t[i], t[i + 8]);
#pragma unroll
      for (int i = 0; i < 8; ++i) u[i] += dpp<0x128>(u[i]);
      const int bb = lane >> 4, hi4 = (lane >> 3) & 1;
      const f32x4 v = {hi4 ? u[4] : u[0], hi4 ? u[5] : u[1], hi4 ? u[6] : u[2], hi4 ? u[7] : u[3]};
      const int off = hw < 3 ? (T0Q_OFF + (((h * 4 + bb) * 8 + j) * 192 + hw * 64 + c * 8 + hi4 * 4) * 4)
                             : (T0H_OFF + (((h * 8 + j) * 4 + bb) * 48 + c * 8 + hi4 * 4) * 4);
      if (hw < 3 || c < 6) STSC(__builtin_bit_cast(u32x4, v), rs, off);
    }
    asm volatile("s_waitcnt vmcnt(0)" ::: "memory");
    {
      unsigned old4 = 0;
      if (lane == 0) old4 = __hip_atomic_fetch_add((LAS unsigned*)(lds + L_MISC + C_DR), 1u, __ATOMIC_RELAXED, __HIP_MEMORY_SCOPE_WORKGROUP);
      if (__builtin_amdgcn_readfirstlane(old4) == 3u && lane < 8) STSC((u32x4{tag, tag, tag, tag}), rs, T0F_OFF + (h * 8 + j) * 128 + lane * 16);
    }
    if (hw == 0) {
      __builtin_amdgcn_s_sleep(T0DELAY);
      const int so = T0F_OFF + (h * 8 + (lane & 7)) * 128;
      bool ok = lane >= 8;
      for (unsigned spin = 0; spin < SPIN_CAP; ++spin) {
        if (!ok) { const u32x4 sn = LDSC(rs, so); ok = okunit(sn, tag); }
        if (__all(ok)) break;
        __builtin_amdgcn_s_sleep(T0SLEEP);
      }
      if (lane == 0) ctl_st(lds, C_T0, 1u);
    } else if (hw == 2) {
      u32x4 G[10];
      const int l16 = lane < 16 ? lane + 64 : lane;
      G[0] = *(const GAS u32x4*)(gw + lane * 8); G[1] = *(const GAS u32x4*)(gw + l16 * 8);
#pragma unroll
      for (int bb = 0; bb < 4; ++bb) { G[2 + 2 * bb] = *(const GAS u32x4*)(x + bb * DM + lane * 8); G[3 + 2 * bb] = *(const GAS u32x4*)(x + bb * DM + l16 * 8); }
#pragma unroll
      for (int bb = 0; bb < 4; ++bb) {
        float s0 = 0.f, s1 = 0.f;
#pragma unroll
        for (int d = 0; d < 4; ++d) {
          s0 += rbf(bf_lo(G[2 + 2 * bb][d]) * bf_lo(G[0][d])); s0 += rbf(bf_hi(G[2 + 2 * bb][d]) * bf_hi(G[0][d]));
          s1 += rbf(bf_lo(G[3 + 2 * bb][d]) * bf_lo(G[1][d])); s1 += rbf(bf_hi(G[3 + 2 * bb][d]) * bf_hi(G[1][d]));
        }
        float t = s0 + (lane < 16 ? s1 : 0.f);
        t += dpp<0xB1>(t); t += dpp<0x4E>(t); t += dpp<0x141>(t); t += dpp<0x140>(t);
        t = swap16_add(t, t); t = swap32_add(t, t);
        t = rbf(t);
        const float g = rbf(1.f / (1.f + __expf(-t)));
        if (lane == 0) { *(float*)(lds + L_MISC + C_G + bb * 4) = g; *(float*)(lds + L_MISC + C_OMG + bb * 4) = rbf(1.f - g); }
      }
    } else if (hw == 3) {
      u32x4 W2[8];
      const int l60 = lane < 60 ? lane : 59, rb = l60 / 10, cq = l60 - rb * 10;
      const unsigned short* p2 = w2 + (size_t)(48 * h + 8 * rb) * DM + 80 * j + cq * 8;
#pragma unroll
      for (int i = 0; i < 8; ++i) W2[i] = ldnt(p2 + i * DM);
      if (lane < 60) {
        const int ks = rb >> 2, kg = rb & 3, nt = cq >> 1;
        const int dst = L_W2B + ((ks * 5 + nt) * 64 + kg * 16 + (cq & 1) * 8 + (cq & 7)) * 16;
#pragma unroll
        for (int cc = 0; cc < 8; ++cc) {
          const unsigned sel = (cc & 1) ? 0x07060302u : 0x05040100u;
          u32x4 o;
#pragma unroll
          for (int kk = 0; kk < 4; ++kk) o[kk] = __builtin_amdgcn_perm(W2[2 * kk + 1][cc >> 1], W2[2 * kk][cc >> 1], sel);
          *(u32x4*)(lds + (dst ^ (cc * 16))) = o;
        }
      }
#pragma unroll
      for (int i = 0; i < 3; ++i) { const int id = lane + 64 * i; if (id < 160) *(u32x4*)(lds + L_W2B + ((5 + id / 32) * 64 + 32 + (id & 31)) * 16) = u32x4{0u, 0u, 0u, 0u}; }
      if (lane < 32) *(unsigned*)(lds + L_HA + (lane >> 3) * 144 + (24 + (lane & 7)) * 4) = 0u;
    }
    if (hw == 0 || hw == 2) {
      ctl_wait<2>(lds, C_T0, 1u);
      __builtin_amdgcn_fence(__ATOMIC_ACQUIRE, "wavefront");
      const int l48 = lane < 48 ? lane : 47;
      const int hb = l48 / 12, hq = l48 - hb * 12;
      const int off0 = hw == 0 ? (T0Q_OFF + ((h * 4 + b) * 8 * 192 + l48 * 4) * 4) : (T0H_OFF + ((h * 8 * 4 + hb) * 48 + hq * 4) * 4);
      u32x4 U[8];
#pragma unroll
      for (int jj = 0; jj < 8; ++jj) U[jj] = LDSC(rs, off0 + jj * 768);
      f32x4 v = __builtin_bit_cast(f32x4, U[0]);
#pragma unroll
      for (int jj = 1; jj < 8; ++jj) v += __builtin_bit_cast(f32x4, U[jj]);
      if (hw == 0) {
        if (lane < 48) *(u32x2*)(lds + L_QS + lane * 8) = u32x2{cvtpk(v[0], v[1]), cvtpk(v[2], v[3])};
        ctl_inc(lds, C_Q, lane);
      } else {
        if (lane < 48) *(u32x2*)(lds + L_HA + hb * 144 + hq * 8) = u32x2{cvtpk(fmaxf(v[0], 0.f), fmaxf(v[1], 0.f)), cvtpk(fmaxf(v[2], 0.f), fmaxf(v[3], 0.f))};
      }
    }
    if (hw >= 2) ctl_inc(lds, C_FFN, lane);
    ctl_wait<4>(lds, C_VISS, 8u);
    {
      u32x4 WO[8];
      const int l60 = lane < 60 ? lane : 59, rbl = l60 / 10, cq = l60 - rbl * 10;
      const unsigned short* po = wo + (size_t)(192 * h + 8 * (6 * hw + rbl)) * DM + 80 * j + cq * 8;
#pragma unroll
      for (int i = 0; i < 8; ++i) { WO[i] = ldnt(po + i * DM); SB(); }
      if (lane < 60) {
        const int rb = 6 * hw + rbl, ks = rb >> 2, kg = rb & 3, nt = cq >> 1;
        const int dst = L_WB + ((ks * 5 + nt) * 64 + kg * 16 + (cq & 1) * 8 + (cq & 7)) * 16;
#pragma unroll
        for (int cc = 0; cc < 8; ++cc) {
          const unsigned sel = (cc & 1) ? 0x07060302u : 0x05040100u;
          u32x4 o;
#pragma unroll
          for (int kk = 0; kk < 4; ++kk) o[kk] = __builtin_amdgcn_perm(WO[2 * kk + 1][cc >> 1], WO[2 * kk][cc >> 1], sel);
          *(u32x4*)(lds + (dst ^ (cc * 16))) = o;
        }
      }
    }
    ctl_inc(lds, C_TILE, lane);
    outproj_tile(lds, rs, hw, lane, h, j, tag);
  }
}

extern "C" void kernel_launch(void* const* d_in, const int* in_sizes, int n_in, void* d_out, int out_size, void* d_ws, size_t ws_size, hipStream_t stream) {
  fused_kernel<<<256, NTHR, 0, stream>>>((const unsigned short*)d_in[0], (const unsigned short*)d_in[1], (const unsigned short*)d_in[2],
      (const unsigned short*)d_in[3], (const unsigned short*)d_in[4], (const unsigned short*)d_in[6], (const unsigned short*)d_in[7],
      (const unsigned short*)d_in[8], (unsigned short*)d_out, (unsigned char*)d_ws);
}
```
